```python
import torch

def _softmax(x, axis=-1):
    return torch.softmax(x, dim=axis)
def _gelu(x):
    return torch.nn.functional.gelu(x, approximate="tanh")
def _silu(x):
    return torch.nn.functional.silu(x)
def _sigmoid(x):
    return torch.sigmoid(x)
def _layernorm(x, axis=-1, eps=1e-5):
    m = x.mean(dim=axis, keepdim=True)
    v = x.var(dim=axis, keepdim=True, unbiased=False)
    return (x - m) / torch.sqrt(v + eps)
def _scatter_add(tgt, idx, src, axis=0):
    return tgt.clone().index_add_(axis, idx, src)
def _segment_sum(src, seg, n):
    out = torch.zeros((n,) + src.shape[1:], dtype=src.dtype)
    return out.index_add_(0, seg, src)
def _pad(x, pw):
    flat = tuple(v for pair in reversed(pw) for v in pair)
    return torch.nn.functional.pad(x, flat)

kernel_name = 'parallel_sum__gqa_block_cached__dense_activation_relu__decode'

def setup_inputs():

    _w = (lambda a: (lambda t: t.to(torch.bfloat16) if t.is_floating_point() else t)(torch.as_tensor(a)))
    def _A_setup():
        B, Sq, D = 1, 1, 512
        Hq, Hkv, Sk, Dh = 32, 8, 65536, 128
        assert Hq % Hkv == 0, (Hq, Hkv)
        return {
            "x": _w((lambda *s: torch.randn(*s, dtype=torch.bfloat16) * 0.05)(B, Sq, D)),
            "k_cache": _w((lambda *s: torch.randn(*s, dtype=torch.bfloat16) * 3.2)(B, Hkv, Sk, Dh)),
            "v_cache": _w((lambda *s: torch.randn(*s, dtype=torch.bfloat16) * 1.6)(B, Hkv, Sk, Dh)),
            "wq": _w((lambda *s: torch.randn(*s, dtype=torch.bfloat16) * 3.2)(D, Hq * Dh) * (D ** -0.5)),
            "wo": _w((lambda *s: torch.randn(*s, dtype=torch.bfloat16) * 1.6)(Hq * Dh, D) * ((Hq * Dh) ** -0.5)),
        }
    _A_in = _A_setup()

    def _B_setup():
        B, S, D, Dff = 1, 1, 512, 1792
        return {
            "x": _w((lambda *s: torch.randn(*s, dtype=torch.bfloat16) * 0.05)(B, S, D)),
            "w1": _w((lambda *s: torch.randn(*s, dtype=torch.bfloat16) * 0.05)(D, Dff)),
            "w2": _w((lambda *s: torch.randn(*s, dtype=torch.bfloat16) * 0.05)(Dff, D)),
        }
    _B_in = _B_setup()

    return {
        "A_x": _A_in["x"],
        "A_k_cache": _A_in["k_cache"],
        "A_v_cache": _A_in["v_cache"],
        "A_wq": _A_in["wq"],
        "A_wo": _A_in["wo"],
        "B_x": _B_in["x"],
        "B_w1": _B_in["w1"],
        "B_w2": _B_in["w2"],
    }

def reference(A_x, A_k_cache, A_v_cache, A_wq, A_wo, B_x, B_w1, B_w2):
    def _A_ref(x, k_cache, v_cache, wq, wo):
        B, Sq, D = x.shape
        Hq, Dh = 32, 128
        Hkv = k_cache.shape[1]
        rep = Hq // Hkv
        q = (lambda x, *d: x.permute(*d))((x @ wq).reshape(B, Sq, Hq, Dh), 0, 2, 1, 3)
        k = (lambda x, n, axis: torch.repeat_interleave(x, n, dim=axis))(k_cache, rep, axis=1)
        v = (lambda x, n, axis: torch.repeat_interleave(x, n, dim=axis))(v_cache, rep, axis=1)
        scores = torch.einsum("bhqd,bhkd->bhqk", q, k) * (Dh ** -0.5)
        p = _softmax(scores, axis=-1)
        o = torch.einsum("bhqk,bhkd->bhqd", p, v)
        return (lambda x, *d: x.permute(*d))(o, 0, 2, 1, 3).reshape(B, Sq, Hq * Dh) @ wo

    def _B_ref(x, w1, w2):
        h = torch.einsum("bsd,df->bsf", x, w1)
        zero = torch.zeros_like(h)
        h = torch.maximum(h, zero)
        return torch.einsum("bsf,fd->bsd", h, w2)

    x = A_x
    xn = _layernorm(x, axis=-1)
    return x + _A_ref(xn, A_k_cache, A_v_cache, A_wq, A_wo) + _B_ref(xn, B_w1, B_w2)
```

```cpp
#include <hip/hip_runtime.h>
#include <hip/hip_bf16.h>
#include <stdint.h>

#define MIMIC 0

typedef unsigned v4u __attribute__((ext_vector_type(4)));
typedef float f32x4 __attribute__((ext_vector_type(4)));
typedef __bf16 bf16x8 __attribute__((ext_vector_type(8)));
typedef unsigned short u16;

constexpr int D = 512, HQ = 32, HKV = 8, SK = 65536, DH = 128, DFF = 1792;
constexpr int NSPLIT = 32, NWAVE = 8;

__device__ __forceinline__ float bf2f(u16 v) { return __uint_as_float(((unsigned)v) << 16); }
__device__ __forceinline__ unsigned cvtpk(float lo, float hi) {
  unsigned r; asm volatile("v_cvt_pk_bf16_f32 %0, %1, %2" : "=v"(r) : "v"(lo), "v"(hi)); return r;
}
__device__ __forceinline__ float rbf(float x) { return __uint_as_float(cvtpk(x, x) << 16); }
__device__ __forceinline__ float lo16(unsigned u) { return __uint_as_float(u << 16); }
__device__ __forceinline__ float hi16(unsigned u) { return __uint_as_float(u & 0xffff0000u); }

__device__ void layernorm256(const u16* x, float* xn, float* red) {
  int t = threadIdx.x;
  float a = bf2f(x[t]), b = bf2f(x[t + 256]);
  float s = a + b;
  for (int o = 32; o; o >>= 1) s += __shfl_xor(s, o);
  if ((t & 63) == 0) red[t >> 6] = s;
  __syncthreads();
  float mean = (red[0] + red[1] + red[2] + red[3]) * (1.f / D);
  __syncthreads();
  float da = a - mean, db = b - mean;
  float v = da * da + db * db;
  for (int o = 32; o; o >>= 1) v += __shfl_xor(v, o);
  if ((t & 63) == 0) red[t >> 6] = v;
  __syncthreads();
  float var = (red[0] + red[1] + red[2] + red[3]) * (1.f / D);
  float r = 1.f / sqrtf(var + 1e-5f);
  xn[t] = da * r; xn[t + 256] = db * r;
  __syncthreads();
}

__global__ void __launch_bounds__(256) k_pre(const u16* x, const u16* wq, const u16* w1, u16* q, float* h) {
  __shared__ float xn[D]; __shared__ float red[4]; __shared__ float part[32][64];
  layernorm256(x, xn, red);
  int b = blockIdx.x, t = threadIdx.x;
  const u16* W; int ld, c0;
  if (b < 64) { W = wq; ld = HQ * DH; c0 = b * 64; } else { W = w1; ld = DFF; c0 = (b - 64) * 64; }
  int cg = t & 7, rg = t >> 3;
  float acc[8] = {0};
  for (int i = 0; i < 16; ++i) {
    int r = rg + 32 * i;
    v4u w = *(const v4u*)(W + (size_t)r * ld + c0 + cg * 8);
    float xv = xn[r];
    for (int k = 0; k < 4; ++k) { acc[2 * k] += xv * lo16(w[k]); acc[2 * k + 1] += xv * hi16(w[k]); }
  }
  for (int k = 0; k < 8; ++k) part[rg][cg * 8 + k] = acc[k];
  __syncthreads();
  if (t < 64) {
    float s = 0; for (int i = 0; i < 32; ++i) s += part[i][t];
    if (b < 64) q[c0 + t] = (u16)(cvtpk(s, s) & 0xffff);
    else { float hv = fmaxf(s, 0.f); h[c0 + t] = MIMIC ? rbf(hv) : hv; }
  }
}

__global__ void __launch_bounds__(512, 2) k_attn(const u16* __restrict__ K, const u16* __restrict__ V, const u16* __restrict__ q, float* pml, float* po) {
  __shared__ float sm[NWAVE][4], sl[NWAVE][4], so[NWAVE][4][DH];
  int g = blockIdx.x & 7, j = blockIdx.x >> 3;
  int lane = threadIdx.x & 63, wave = __builtin_amdgcn_readfirstlane(threadIdx.x >> 6);
  int grp = lane >> 4, i = lane & 15;
  v4u qf[4];
  for (int s = 0; s < 4; ++s) qf[s] = *(const v4u*)(q + (g * 4 + (i & 3)) * DH + 32 * s + 8 * grp);
  const u16* Kb = K + (size_t)g * SK * DH; const u16* Vb = V + (size_t)g * SK * DH;
  constexpr int KEYS_PER_WG = SK / NSPLIT, TILES = KEYS_PER_WG / 32 / NWAVE;
  f32x4 acc[8]; for (int d = 0; d < 8; ++d) acc[d] = f32x4{0, 0, 0, 0};
  float m = -INFINITY, l = 0.f;
  const float C2 = 0.08838834764831845f * 1.4426950408889634f;
  int krow = 4 * (i & 3) + (i >> 2);
  for (int t = 0; t < TILES; ++t) {
    int key0 = j * KEYS_PER_WG + (t * NWAVE + wave) * 32;
    v4u kf[2][4], vf[8];
    for (int u = 0; u < 2; ++u) for (int s = 0; s < 4; ++s)
      kf[u][s] = __builtin_nontemporal_load((const v4u*)(Kb + (size_t)(key0 + 16 * u + krow) * DH + 32 * s + 8 * grp));
    for (int u = 0; u < 2; ++u) for (int r = 0; r < 4; ++r)
      vf[u * 4 + r] = __builtin_nontemporal_load((const v4u*)(Vb + (size_t)(key0 + 16 * u + 4 * r + grp) * DH + 8 * i));
    f32x4 sc[2];
    for (int u = 0; u < 2; ++u) { sc[u] = f32x4{0, 0, 0, 0};
      for (int s = 0; s < 4; ++s) sc[u] = __builtin_amdgcn_mfma_f32_16x16x32_bf16(__builtin_bit_cast(bf16x8, kf[u][s]), __builtin_bit_cast(bf16x8, qf[s]), sc[u], 0, 0, 0); }
    float x[8];
    for (int u = 0; u < 2; ++u) for (int r = 0; r < 4; ++r) {
      x[u * 4 + r] = sc[u][r] * C2;
    }
    float tm = x[0]; for (int k = 1; k < 8; ++k) tm = fmaxf(tm, x[k]);
    tm = fmaxf(tm, __shfl_xor(tm, 16)); tm = fmaxf(tm, __shfl_xor(tm, 32));
    float mn = fmaxf(m, tm), alpha = __builtin_amdgcn_exp2f(m - mn); m = mn;
    float p[8], ps = 0; for (int k = 0; k < 8; ++k) { p[k] = __builtin_amdgcn_exp2f(x[k] - mn); ps += p[k]; }
    l = l * alpha + ps;
    v4u pf; for (int k = 0; k < 4; ++k) pf[k] = cvtpk(p[2 * k], p[2 * k + 1]);
    for (int d = 0; d < 8; ++d) {
      v4u a;
      for (int k = 0; k < 4; ++k) a[k] = __builtin_amdgcn_perm(vf[2 * k + 1][d >> 1], vf[2 * k][d >> 1], (d & 1) ? 0x07060302u : 0x05040100u);
      acc[d] *= alpha;
      acc[d] = __builtin_amdgcn_mfma_f32_16x16x32_bf16(__builtin_bit_cast(bf16x8, a), __builtin_bit_cast(bf16x8, pf), acc[d], 0, 0, 0);
    }
  }
  l += __shfl_xor(l, 16); l += __shfl_xor(l, 32);
  if (i < 4) {
    if (grp == 0) { sm[wave][i] = m; sl[wave][i] = l; }
    for (int d = 0; d < 8; ++d) for (int r = 0; r < 4; ++r) so[wave][i][8 * (4 * grp + r) + d] = acc[d][r];
  }
  __syncthreads();
  int t = threadIdx.x, hh = t >> 7, dd = t & 127;
  float M = -INFINITY; for (int w = 0; w < NWAVE; ++w) M = fmaxf(M, sm[w][hh]);
  float L = 0, O = 0; for (int w = 0; w < NWAVE; ++w) { float e = __builtin_amdgcn_exp2f(sm[w][hh] - M); L += e * sl[w][hh]; O += e * so[w][hh][dd]; }
  int slot = (g * 4 + hh) * NSPLIT + j;
  po[(size_t)slot * DH + dd] = O;
  if (dd == 0) { pml[slot * 2] = M; pml[slot * 2 + 1] = L; }
}

__global__ void __launch_bounds__(256) k_post(const float* pml, const float* po, const float* h, const u16* wo, const u16* w2, float* partA, float* partB) {
  __shared__ float o[16];
  int b = blockIdx.x, t = threadIdx.x, hq = b >> 3, d0 = (b & 7) * 16;
  if (t < 16) {
    float M = -INFINITY; for (int s = 0; s < NSPLIT; ++s) M = fmaxf(M, pml[(hq * NSPLIT + s) * 2]);
    float L = 0, O = 0;
    for (int s = 0; s < NSPLIT; ++s) { float e = __builtin_amdgcn_exp2f(pml[(hq * NSPLIT + s) * 2] - M); L += e * pml[(hq * NSPLIT + s) * 2 + 1]; O += e * po[(size_t)(hq * NSPLIT + s) * DH + d0 + t]; }
    o[t] = MIMIC ? rbf(O / L) : O / L;
  }
  __syncthreads();
  float a0 = 0, a1 = 0, b0 = 0, b1 = 0;
  for (int r = 0; r < 16; ++r) { unsigned w = *(const unsigned*)(wo + (size_t)(16 * b + r) * D + 2 * t); a0 += o[r] * lo16(w); a1 += o[r] * hi16(w); }
  for (int r = 0; r < 7; ++r) { unsigned w = *(const unsigned*)(w2 + (size_t)(7 * b + r) * D + 2 * t); float hv = h[7 * b + r]; b0 += hv * lo16(w); b1 += hv * hi16(w); }
  partA[b * D + 2 * t] = a0; partA[b * D + 2 * t + 1] = a1;
  partB[b * D + 2 * t] = b0; partB[b * D + 2 * t + 1] = b1;
}

__global__ void __launch_bounds__(256) k_fin(const float* partA, const float* partB, const u16* x, u16* out) {
  __shared__ float ra[4][64], rb[4][64];
  int c = blockIdx.x * 64 + (threadIdx.x & 63), w = threadIdx.x >> 6;
  float a = 0, b = 0;
  for (int p = w; p < 256; p += 4) { a += partA[p * D + c]; b += partB[p * D + c]; }
  ra[w][threadIdx.x & 63] = a; rb[w][threadIdx.x & 63] = b;
  __syncthreads();
  if (w == 0) {
    int k = threadIdx.x;
    a = ra[0][k] + ra[1][k] + ra[2][k] + ra[3][k]; b = rb[0][k] + rb[1][k] + rb[2][k] + rb[3][k];
    float r = MIMIC ? rbf(bf2f(x[c]) + rbf(a)) + rbf(b) : bf2f(x[c]) + a + b;
    out[c] = (u16)(cvtpk(r, r) & 0xffff);
  }
}

extern "C" void kernel_launch(void* const* d_in, const int* in_sizes, int n_in, void* d_out, int out_size, void* d_ws, size_t ws_size, hipStream_t stream) {
  const u16* x = (const u16*)d_in[0]; const u16* K = (const u16*)d_in[1]; const u16* V = (const u16*)d_in[2];
  const u16* wq = (const u16*)d_in[3]; const u16* wo = (const u16*)d_in[4]; const u16* w1 = (const u16*)d_in[6]; const u16* w2 = (const u16*)d_in[7];
  char* ws = (char*)d_ws;
  u16* q = (u16*)ws; float* h = (float*)(ws + 8192); float* pml = (float*)(ws + 16384); float* po = (float*)(ws + 32768);
  float* partA = (float*)(ws + 32768 + 524288); float* partB = partA + 256 * 512;
  k_pre<<<92, 256, 0, stream>>>(x, wq, w1, q, h);
  k_attn<<<256, 512, 0, stream>>>(K, V, q, pml, po);
  k_post<<<256, 256, 0, stream>>>(pml, po, h, wo, w2, partA, partB);
  k_fin<<<8, 256, 0, stream>>>(partA, partB, x, (u16*)d_out);
}
```

```cpp
#include <hip/hip_runtime.h>
#include <hip/hip_bf16.h>
#include <stdint.h>

#define MIMIC 1

typedef unsigned v4u __attribute__((ext_vector_type(4)));
typedef float f32x4 __attribute__((ext_vector_type(4)));
typedef __bf16 bf16x8 __attribute__((ext_vector_type(8)));
typedef unsigned short u16;

constexpr int D = 512, HQ = 32, HKV = 8, SK = 65536, DH = 128, DFF = 1792;
constexpr int NSPLIT = 32, NWAVE = 8;

__device__ __forceinline__ float bf2f(u16 v) { return __uint_as_float(((unsigned)v) << 16); }
typedef __bf16 bf16x2 __attribute__((ext_vector_type(2)));
typedef float f32x2 __attribute__((ext_vector_type(2)));
__device__ __forceinline__ unsigned cvtpk(float lo, float hi) {
  return __builtin_bit_cast(unsigned, __builtin_convertvector(f32x2{lo, hi}, bf16x2));
}
__device__ __forceinline__ float rbf(float x) { return __uint_as_float(cvtpk(x, x) << 16); }
__device__ __forceinline__ float lo16(unsigned u) { return __uint_as_float(u << 16); }
__device__ __forceinline__ float hi16(unsigned u) { return __uint_as_float(u & 0xffff0000u); }

__device__ void layernorm256(const u16* x, float* xn, float* red) {
  int t = threadIdx.x;
  float a = bf2f(x[t]), b = bf2f(x[t + 256]);
  float s = a + b;
  for (int o = 32; o; o >>= 1) s += __shfl_xor(s, o);
  if ((t & 63) == 0) red[t >> 6] = s;
  __syncthreads();
  float mean = (red[0] + red[1] + red[2] + red[3]) * (1.f / D);
  __syncthreads();
  float da = a - mean, db = b - mean;
  float v = da * da + db * db;
  for (int o = 32; o; o >>= 1) v += __shfl_xor(v, o);
  if ((t & 63) == 0) red[t >> 6] = v;
  __syncthreads();
  float var = (red[0] + red[1] + red[2] + red[3]) * (1.f / D);
  float mb = rbf(mean), vb = rbf(var);
  float den = rbf(sqrtf(rbf(vb + 1e-5f)));
  xn[t] = rbf(rbf(a - mb) / den);
  xn[t + 256] = rbf(rbf(b - mb) / den);
  __syncthreads();
}

__global__ void __launch_bounds__(256) k_pre(const u16* x, const u16* wq, const u16* w1, u16* q, float* h) {
  __shared__ float xn[D]; __shared__ float red[4]; __shared__ float part[32][64];
  layernorm256(x, xn, red);
  int b = blockIdx.x, t = threadIdx.x;
  const u16* W; int ld, c0;
  if (b < 64) { W = wq; ld = HQ * DH; c0 = b * 64; } else { W = w1; ld = DFF; c0 = (b - 64) * 64; }
  int cg = t & 7, rg = t >> 3;
  float acc[8] = {0};
  for (int i = 0; i < 16; ++i) {
    int r = rg + 32 * i;
    v4u w = *(const v4u*)(W + (size_t)r * ld + c0 + cg * 8);
    float xv = xn[r];
    for (int k = 0; k < 4; ++k) { acc[2 * k] += xv * lo16(w[k]); acc[2 * k + 1] += xv * hi16(w[k]); }
  }
  for (int k = 0; k < 8; ++k) part[rg][cg * 8 + k] = acc[k];
  __syncthreads();
  if (t < 64) {
    float s = 0; for (int i = 0; i < 32; ++i) s += part[i][t];
    if (b < 64) q[c0 + t] = (u16)(cvtpk(s, s) & 0xffff);
    else { float hv = fmaxf(s, 0.f); h[c0 + t] = MIMIC ? rbf(hv) : hv; }
  }
}

__global__ void __launch_bounds__(512, 2) k_attn(const u16* __restrict__ K, const u16* __restrict__ V, const u16* __restrict__ q, float* pml, float* po) {
  __shared__ float sm[NWAVE][4], sl[NWAVE][4], so[NWAVE][4][DH];
  int g = blockIdx.x & 7, j = blockIdx.x >> 3;
  int lane = threadIdx.x & 63, wave = __builtin_amdgcn_readfirstlane(threadIdx.x >> 6);
  int grp = lane >> 4, i = lane & 15;
  v4u qf[4];
  for (int s = 0; s < 4; ++s) qf[s] = *(const v4u*)(q + (g * 4 + (i & 3)) * DH + 32 * s + 8 * grp);
  const u16* Kb = K + (size_t)g * SK * DH; const u16* Vb = V + (size_t)g * SK * DH;
  constexpr int KEYS_PER_WG = SK / NSPLIT, TILES = KEYS_PER_WG / 32 / NWAVE;
  f32x4 acc[8]; for (int d = 0; d < 8; ++d) acc[d] = f32x4{0, 0, 0, 0};
  float m = -INFINITY, l = 0.f;
  const float C2 = 0.08838834764831845f * 1.4426950408889634f;
  int krow = 4 * (i & 3) + (i >> 2);
  for (int t = 0; t < TILES; ++t) {
    int key0 = j * KEYS_PER_WG + (t * NWAVE + wave) * 32;
    v4u kf[2][4], vf[8];
    for (int u = 0; u < 2; ++u) for (int s = 0; s < 4; ++s)
      kf[u][s] = __builtin_nontemporal_load((const v4u*)(Kb + (size_t)(key0 + 16 * u + krow) * DH + 32 * s + 8 * grp));
    for (int u = 0; u < 2; ++u) for (int r = 0; r < 4; ++r)
      vf[u * 4 + r] = __builtin_nontemporal_load((const v4u*)(Vb + (size_t)(key0 + 16 * u + 4 * r + grp) * DH + 8 * i));
    f32x4 sc[2];
    for (int u = 0; u < 2; ++u) { sc[u] = f32x4{0, 0, 0, 0};
      for (int s = 0; s < 4; ++s) sc[u] = __builtin_amdgcn_mfma_f32_16x16x32_bf16(__builtin_bit_cast(bf16x8, kf[u][s]), __builtin_bit_cast(bf16x8, qf[s]), sc[u], 0, 0, 0); }
    float x[8];
    for (int u = 0; u < 2; ++u) for (int r = 0; r < 4; ++r) {
      x[u * 4 + r] = rbf(rbf(sc[u][r]) * 0.08838834764831845f) * 1.4426950408889634f;
    }
    float tm = x[0]; for (int k = 1; k < 8; ++k) tm = fmaxf(tm, x[k]);
    tm = fmaxf(tm, __shfl_xor(tm, 16)); tm = fmaxf(tm, __shfl_xor(tm, 32));
    float mn = fmaxf(m, tm), alpha = __builtin_amdgcn_exp2f(m - mn); m = mn;
    float p[8], ps = 0; for (int k = 0; k < 8; ++k) { p[k] = __builtin_amdgcn_exp2f(x[k] - mn); ps += p[k]; }
    l = l * alpha + ps;
    v4u pf; for (int k = 0; k < 4; ++k) pf[k] = cvtpk(p[2 * k], p[2 * k + 1]);
    for (int d = 0; d < 8; ++d) {
      v4u a;
      for (int k = 0; k < 4; ++k) a[k] = __builtin_amdgcn_perm(vf[2 * k + 1][d >> 1], vf[2 * k][d >> 1], (d & 1) ? 0x07060302u : 0x05040100u);
      acc[d] *= alpha;
      acc[d] = __builtin_amdgcn_mfma_f32_16x16x32_bf16(__builtin_bit_cast(bf16x8, a), __builtin_bit_cast(bf16x8, pf), acc[d], 0, 0, 0);
    }
  }
  l += __shfl_xor(l, 16); l += __shfl_xor(l, 32);
  if (i < 4) {
    if (grp == 0) { sm[wave][i] = m; sl[wave][i] = l; }
    for (int d = 0; d < 8; ++d) for (int r = 0; r < 4; ++r) so[wave][i][8 * (4 * grp + r) + d] = acc[d][r];
  }
  __syncthreads();
  int t = threadIdx.x, hh = t >> 7, dd = t & 127;
  float M = -INFINITY; for (int w = 0; w < NWAVE; ++w) M = fmaxf(M, sm[w][hh]);
  float L = 0, O = 0; for (int w = 0; w < NWAVE; ++w) { float e = __builtin_amdgcn_exp2f(sm[w][hh] - M); L += e * sl[w][hh]; O += e * so[w][hh][dd]; }
  int slot = (g * 4 + hh) * NSPLIT + j;
  po[(size_t)slot * DH + dd] = O;
  if (dd == 0) { pml[slot * 2] = M; pml[slot * 2 + 1] = L; }
}

__global__ void __launch_bounds__(256) k_post(const float* pml, const float* po, const float* h, const u16* wo, const u16* w2, float* partA, float* partB) {
  __shared__ float o[16];
  int b = blockIdx.x, t = threadIdx.x, hq = b >> 3, d0 = (b & 7) * 16;
  if (t < 16) {
    float M = -INFINITY; for (int s = 0; s < NSPLIT; ++s) M = fmaxf(M, pml[(hq * NSPLIT + s) * 2]);
    float L = 0, O = 0;
    for (int s = 0; s < NSPLIT; ++s) { float e = __builtin_amdgcn_exp2f(pml[(hq * NSPLIT + s) * 2] - M); L += e * pml[(hq * NSPLIT + s) * 2 + 1]; O += e * po[(size_t)(hq * NSPLIT + s) * DH + d0 + t]; }
    o[t] = MIMIC ? rbf(O / L) : O / L;
  }
  __syncthreads();
  float a0 = 0, a1 = 0, b0 = 0, b1 = 0;
  for (int r = 0; r < 16; ++r) { unsigned w = *(const unsigned*)(wo + (size_t)(16 * b + r) * D + 2 * t); a0 += o[r] * lo16(w); a1 += o[r] * hi16(w); }
  for (int r = 0; r < 7; ++r) { unsigned w = *(const unsigned*)(w2 + (size_t)(7 * b + r) * D + 2 * t); float hv = h[7 * b + r]; b0 += hv * lo16(w); b1 += hv * hi16(w); }
  partA[b * D + 2 * t] = a0; partA[b * D + 2 * t + 1] = a1;
  partB[b * D + 2 * t] = b0; partB[b * D + 2 * t + 1] = b1;
}

__global__ void __launch_bounds__(256) k_fin(const float* partA, const float* partB, const u16* x, u16* out) {
  __shared__ float ra[4][64], rb[4][64];
  int c = blockIdx.x * 64 + (threadIdx.x & 63), w = threadIdx.x >> 6;
  float a = 0, b = 0;
  for (int p = w; p < 256; p += 4) { a += partA[p * D + c]; b += partB[p * D + c]; }
  ra[w][threadIdx.x & 63] = a; rb[w][threadIdx.x & 63] = b;
  __syncthreads();
  if (w == 0) {
    int k = threadIdx.x;
    a = ra[0][k] + ra[1][k] + ra[2][k] + ra[3][k]; b = rb[0][k] + rb[1][k] + rb[2][k] + rb[3][k];
    float r = MIMIC ? rbf(bf2f(x[c]) + rbf(a)) + rbf(b) : bf2f(x[c]) + a + b;
    out[c] = (u16)(cvtpk(r, r) & 0xffff);
  }
}

extern "C" void kernel_launch(void* const* d_in, const int* in_sizes, int n_in, void* d_out, int out_size, void* d_ws, size_t ws_size, hipStream_t stream) {
  const u16* x = (const u16*)d_in[0]; const u16* K = (const u16*)d_in[1]; const u16* V = (const u16*)d_in[2];
  const u16* wq = (const u16*)d_in[3]; const u16* wo = (const u16*)d_in[4]; const u16* w1 = (const u16*)d_in[6]; const u16* w2 = (const u16*)d_in[7];
  char* ws = (char*)d_ws;
  u16* q = (u16*)ws; float* h = (float*)(ws + 8192); float* pml = (float*)(ws + 16384); float* po = (float*)(ws + 32768);
  float* partA = (float*)(ws + 32768 + 524288); float* partB = partA + 256 * 512;
  k_pre<<<92, 256, 0, stream>>>(x, wq, w1, q, h);
  k_attn<<<256, 512, 0, stream>>>(K, V, q, pml, po);
  k_post<<<256, 256, 0, stream>>>(pml, po, h, wo, w2, partA, partB);
  k_fin<<<8, 256, 0, stream>>>(partA, partB, x, (u16*)d_out);
}
```

```cpp
#include <hip/hip_runtime.h>
#include <stdint.h>

#define DBG 0
#define WIN 20
#define AUXW 2

#define LAS __attribute__((address_space(3)))
typedef unsigned v4u __attribute__((ext_vector_type(4)));
typedef unsigned v2u __attribute__((ext_vector_type(2)));
typedef float f32x4 __attribute__((ext_vector_type(4)));
typedef float f32x2 __attribute__((ext_vector_type(2)));
typedef __bf16 bf16x8 __attribute__((ext_vector_type(8)));
typedef __bf16 bf16x2 __attribute__((ext_vector_type(2)));
typedef short s16x4 __attribute__((ext_vector_type(4)));
typedef unsigned short u16;

constexpr int D = 512, HQ = 32, HKV = 8, SK = 65536, DH = 128, DFF = 1792;
constexpr int NWG = 256, NTHR = 320, NTILE = 64;
constexpr int NS = 9, SLOT = 16384;
constexpr int L_XN = NS * SLOT, L_QB = L_XN + 1024, L_MRG = L_QB + 1024, L_ML = L_MRG + 8192, L_OV = L_ML + 128, L_RED = L_OV + 128,
              L_FULL = L_RED + 64, L_FREE = L_FULL + 256, L_QRDY = L_FREE + 64, L_END = L_QRDY + 64;
static_assert(L_END <= 163840, "LDS");
constexpr int W_E = 0, W_Q = 4096, W_ML = 32768, W_O = 65536, W_P = W_O + (1 << 20), W_END = W_P + (1 << 20), W_DBG = 4 << 20;
constexpr unsigned SPIN_CAP = 1u << 16;

__device__ __forceinline__ unsigned cvtpk(float lo, float hi) { return __builtin_bit_cast(unsigned, __builtin_convertvector(f32x2{lo, hi}, bf16x2)); }
__device__ __forceinline__ float lo16(unsigned u) { return __uint_as_float(u << 16); }
__device__ __forceinline__ float hi16(unsigned u) { return __uint_as_float(u & 0xffff0000u); }
__device__ __forceinline__ float rbf(float x) { return lo16(cvtpk(x, x)); }
template <int N> __device__ __forceinline__ void wait_vm() { asm volatile("s_waitcnt vmcnt(%0)" ::"n"(N) : "memory"); }
template <int CTRL> __device__ __forceinline__ float dpp(float x) { return __builtin_bit_cast(float, __builtin_amdgcn_mov_dpp(__builtin_bit_cast(int, x), CTRL, 0xf, 0xf, true)); }
constexpr int XOR1 = 0xB1, XOR2 = 0x4E, XOR7 = 0x141, ROR8 = 0x128, ROR4 = 0x124;
__device__ __forceinline__ float x16_32_max(float x) {
  auto s = __builtin_amdgcn_permlane16_swap(__float_as_uint(x), __float_as_uint(x), false, false);
  x = fmaxf(__uint_as_float(s[0]), __uint_as_float(s[1]));
  auto t = __builtin_amdgcn_permlane32_swap(__float_as_uint(x), __float_as_uint(x), false, false);
  return fmaxf(__uint_as_float(t[0]), __uint_as_float(t[1]));
}
__device__ __forceinline__ float x16_32_sum(float x) {
  auto s = __builtin_amdgcn_permlane16_swap(__float_as_uint(x), __float_as_uint(x), false, false);
  x = __uint_as_float(s[0]) + __uint_as_float(s[1]);
  auto t = __builtin_amdgcn_permlane32_swap(__float_as_uint(x), __float_as_uint(x), false, false);
  return __uint_as_float(t[0]) + __uint_as_float(t[1]);
}
__device__ __forceinline__ float wave_sum(float x) {
  x += dpp<XOR1>(x); x += dpp<XOR2>(x); x += dpp<XOR7>(x); x += dpp<ROR8>(x);
  return x16_32_sum(x);
}
__device__ __forceinline__ f32x4 mfma(v4u a, v4u b, f32x4 c) { return __builtin_amdgcn_mfma_f32_16x16x32_bf16(__builtin_bit_cast(bf16x8, a), __builtin_bit_cast(bf16x8, b), c, 0, 0, 0); }
__device__ __forceinline__ v2u trread(LAS char* p) { return __builtin_bit_cast(v2u, __builtin_amdgcn_ds_read_tr16_b64_v4i16((LAS s16x4*)p)); }
__device__ __forceinline__ void bar() { asm volatile("s_waitcnt lgkmcnt(0)\n\ts_barrier" ::: "memory"); }
__device__ __forceinline__ void lds_wait_ge(LAS char* L, int off, unsigned need) {
  volatile LAS unsigned* p = (volatile LAS unsigned*)(L + off);
  _Pragma("unroll 1") for (unsigned n = 0; n < SPIN_CAP; ++n) {
    if (__builtin_amdgcn_readfirstlane(*p) >= need) break;
    __builtin_amdgcn_s_sleep(1);
  }
  asm volatile("" ::: "memory");
}
#define STAMP(k) do {} while (0)

__global__ void __launch_bounds__(NTHR) fused_decode(const u16* __restrict__ x, const u16* __restrict__ Kc, const u16* __restrict__ Vc, const u16* __restrict__ wq,
                                                     const u16* __restrict__ wo, const u16* __restrict__ w1, const u16* __restrict__ w2, u16* __restrict__ out, char* ws) {
  extern __shared__ __attribute__((aligned(16))) char lds_raw[];
  LAS char* L = (LAS char*)lds_raw;
  const int b = blockIdx.x, lane = threadIdx.x & 63, wave = __builtin_amdgcn_readfirstlane(threadIdx.x >> 6);
  const int xcd = b & 7, r32 = b >> 3;
  const int g = xcd, j = r32;
  const int bb = g * 32 + j;

  if (wave == 0) {
    __builtin_amdgcn_s_setprio(3);
    auto rk = __builtin_amdgcn_make_buffer_rsrc((void*)Kc, 0, HKV * SK * DH * 2, 0x00020000);
    auto rv = __builtin_amdgcn_make_buffer_rsrc((void*)Vc, 0, HKV * SK * DH * 2, 0x00020000);
    auto rq = __builtin_amdgcn_make_buffer_rsrc((void*)wq, 0, D * HQ * DH * 2, 0x00020000);
    {
      const int vo = (lane >> 1) * (HQ * DH * 2) + (lane & 1) * 16, so = bb * 32;
#pragma unroll
      for (int u = 0; u < 16; ++u) __builtin_amdgcn_raw_ptr_buffer_load_lds(rq, L + u * 1024, 16, vo, so + u * 32 * (HQ * DH * 2), 0, AUXW);
    }
    const unsigned fa = L_FULL + lane * 4;
    asm volatile("ds_write_b32 %0, %1\n\tds_write_b32 %0, %1 offset:128\n\ts_waitcnt lgkmcnt(0)\n\ts_barrier" ::"v"(fa), "v"(0u) : "memory");
    const int rr = lane >> 4, p = lane & 15;
    int vk[4], vv[2];
#pragma unroll
    for (int u = 0; u < 4; ++u) vk[u] = rr * 256 + ((p ^ (4 * u + rr)) << 4);
#pragma unroll
    for (int u = 0; u < 2; ++u) vv[u] = rr * 256 + ((p ^ (2 * (4 * u + rr))) << 4);
    int slot = 1;
    for (int z = 1; z <= NTILE; ++z) {
      const int so = (bb * NTILE + z - 1) * 8192;
      LAS char* dst = L + slot * SLOT;
      const int nslot = (slot + 1 == NS) ? 0 : slot + 1;
      unsigned fr;
#pragma unroll
      for (int u = 0; u < 16; ++u) {
        wait_vm<WIN - 1>();
        if (((u - WIN + 1) & 7) == 0) {
          const int v = (16 * z + u - WIN + 1) / 8;
          asm volatile("ds_write_b32 %0, %1" ::"v"(fa), "v"(v < 0 ? 0 : v) : "memory");
        }
        if (u < 8) __builtin_amdgcn_raw_ptr_buffer_load_lds(rk, dst + u * 1024, 16, vk[u & 3], so + u * 1024, 0, 2);
        else __builtin_amdgcn_raw_ptr_buffer_load_lds(rv, dst + u * 1024, 16, vv[u & 1], so + (u - 8) * 1024, 0, 2);
        if (u == 13) asm volatile("ds_read_b32 %0, %1" : "=v"(fr) : "v"(L_FREE + nslot * 4) : "memory");
      }
      asm volatile("s_waitcnt lgkmcnt(0)" : "+v"(fr)::"memory");
      if (z + 1 >= NS) {
        const unsigned need = z + 2 - NS;
        _Pragma("unroll 1") for (unsigned n = 0; n < SPIN_CAP && (unsigned)__builtin_amdgcn_readfirstlane(fr) < need; ++n)
          asm volatile("s_sleep 1\n\tds_read_b32 %0, %1\n\ts_waitcnt lgkmcnt(0)" : "=v"(fr) : "v"(L_FREE + nslot * 4) : "memory");
      }
      slot = nslot;
    }
    {
      auto r1 = __builtin_amdgcn_make_buffer_rsrc((void*)w1, 0, D * DFF * 2, 0x00020000);
      auto r2 = __builtin_amdgcn_make_buffer_rsrc((void*)w2, 0, D * DFF * 2, 0x00020000);
      auto ro = __builtin_amdgcn_make_buffer_rsrc((void*)wo, 0, D * HQ * DH * 2, 0x00020000);
      const int f0 = (xcd * 28 + (r32 < 28 ? r32 : 27)) * 8;
      LAS char* d1 = L + ((NTILE + 1) % NS) * SLOT;
      LAS char* d2 = L + ((NTILE + 2) % NS) * SLOT;
#pragma unroll
      for (int u = 0; u < 8; ++u) __builtin_amdgcn_raw_ptr_buffer_load_lds(r1, d1 + u * 1024, 16, lane * (DFF * 2), f0 * 2 + u * 64 * (DFF * 2), 0, AUXW);
#pragma unroll
      for (int u = 0; u < 8; ++u) __builtin_amdgcn_raw_ptr_buffer_load_lds(r2, d1 + 8192 + u * 1024, 16, lane * 16, (f0 + u) * 1024, 0, 2);
      unsigned fr = 0;
      _Pragma("unroll 1") for (unsigned n = 0; n < SPIN_CAP && (unsigned)__builtin_amdgcn_readfirstlane(fr) < NTILE + 3 - NS; ++n)
        asm volatile("ds_read_b32 %0, %1\n\ts_waitcnt lgkmcnt(0)" : "=v"(fr) : "v"(L_FREE + ((NTILE + 2) % NS) * 4) : "memory");
#pragma unroll
      for (int u = 0; u < 16; ++u) __builtin_amdgcn_raw_ptr_buffer_load_lds(ro, d2 + u * 1024, 16, lane * 16, (bb * 16 + u) * 1024, 0, 2);
    }
    if (WIN > 16) { wait_vm<32 + 16>(); asm volatile("ds_write_b32 %0, %1" ::"v"(fa), "v"(2 * NTILE) : "memory"); }
    wait_vm<32 + 8>(); asm volatile("ds_write_b32 %0, %1" ::"v"(fa), "v"(2 * NTILE + 1) : "memory");
    wait_vm<32>(); asm volatile("ds_write_b32 %0, %1" ::"v"(fa), "v"(2 * NTILE + 2) : "memory");
    STAMP(1);
    bar();
    wait_vm<0>();
    bar();
    return;
  }

  const int cw = wave - 1, t = threadIdx.x - 64, grp = lane >> 4, i = lane & 15;
  if (cw == 0) STAMP(0);
  auto rw = __builtin_amdgcn_make_buffer_rsrc((void*)ws, 0, W_END, 0x00020000);
  const v2u E = __builtin_amdgcn_raw_buffer_load_b64(rw, W_E, 0, 16);
  v4u xv;
  if (cw == 0) xv = *(const v4u*)(x + lane * 8);
  bar();
  const unsigned tagA = E[0] + 1u, tagB = E[1] + 1u;

  if (cw == 0) {
    float xf[8];
#pragma unroll
    for (int k = 0; k < 4; ++k) { xf[2 * k] = lo16(xv[k]); xf[2 * k + 1] = hi16(xv[k]); }
    float s = 0.f;
#pragma unroll
    for (int k = 0; k < 8; ++k) s += xf[k];
    const float mean = wave_sum(s) * (1.f / D);
    float v = 0.f;
#pragma unroll
    for (int k = 0; k < 8; ++k) { const float d = xf[k] - mean; v += d * d; }
    const float var = wave_sum(v) * (1.f / D);
    const float mb = rbf(mean), den = rbf(sqrtf(rbf(rbf(var) + 1e-5f)));
    v4u xn;
#pragma unroll
    for (int k = 0; k < 4; ++k) xn[k] = cvtpk(rbf(xf[2 * k] - mb) / den, rbf(xf[2 * k + 1] - mb) / den);
    *(LAS v4u*)(L + L_XN + lane * 16) = xn;
    lds_wait_ge(L, L_FULL, 2);
    STAMP(2);
    f32x4 a0 = {0.f, 0.f, 0.f, 0.f}, a1 = {0.f, 0.f, 0.f, 0.f};
    LAS char* wp = L + (8 * grp + (i >> 2)) * 32 + (i & 3) * 8;
#pragma unroll
    for (int s2 = 0; s2 < 16; ++s2) {
      const v2u e0 = trread(wp + s2 * 1024), e1 = trread(wp + s2 * 1024 + 128);
      const v4u xb = *(LAS v4u*)(L + L_XN + s2 * 64 + grp * 16);
      if (s2 & 1) a1 = mfma(v4u{e0[0], e0[1], e1[0], e1[1]}, xb, a1);
      else a0 = mfma(v4u{e0[0], e0[1], e1[0], e1[1]}, xb, a0);
    }
    a0 += a1;
    if (i == 0) __builtin_amdgcn_raw_buffer_store_b128(v4u{cvtpk(a0[0], a0[1]), tagA, cvtpk(a0[2], a0[3]), tagB}, rw, W_Q + bb * 64 + grp * 16, 0, 16);
    asm volatile("" ::: "memory");
    if (lane == 0) *(volatile LAS unsigned*)(L + L_FREE) = 1u;
    STAMP(3);
    v4u q0, q1;
    _Pragma("unroll 1") for (unsigned n = 0; n < SPIN_CAP; ++n) {
      q0 = __builtin_amdgcn_raw_buffer_load_b128(rw, W_Q + g * 2048 + lane * 32, 0, 16);
      q1 = __builtin_amdgcn_raw_buffer_load_b128(rw, W_Q + g * 2048 + lane * 32 + 16, 0, 16);
      const bool ok = q0[1] == tagA && q0[3] == tagB && q1[1] == tagA && q1[3] == tagB;
      if (__all(ok)) break;
      asm volatile("s_sleep 1" ::: "memory");
    }
    *(LAS v4u*)(L + L_QB + lane * 16) = v4u{q0[0], q0[2], q1[0], q1[2]};
    asm volatile("s_waitcnt lgkmcnt(0)" ::: "memory");
    if (lane == 0) *(volatile LAS unsigned*)(L + L_QRDY) = 1u;
    STAMP(4);
  }

  v4u qf[4];
  f32x4 acc[8];
#pragma unroll
  for (int n = 0; n < 8; ++n) acc[n] = f32x4{0.f, 0.f, 0.f, 0.f};
  float m = -INFINITY, l = 0.f;
  const int ka0 = i * 256 + ((grp ^ i) << 4);
  const int va0 = 8192 + (4 * grp + (i >> 2)) * 256 + ((((i & 3) >> 1) ^ (2 * (4 * (grp & 1) + (i >> 2)))) << 4) + (i & 1) * 8;
  constexpr float LOG2E = 1.4426950408889634f, SCALE = 0.08838834764831845f;
  int slot = (cw == 0 ? 4 : cw);
  bool first = true;
  for (int z = slot; z <= NTILE; z += 4) {
    LAS char* S = L + slot * SLOT;
    v4u kf[2][4];
    v2u vf[8][2];
    lds_wait_ge(L, L_FULL, 2 * z + 1);
#pragma unroll
    for (int s = 0; s < 4; ++s)
#pragma unroll
      for (int u = 0; u < 2; ++u) kf[u][s] = *(LAS v4u*)(S + (ka0 ^ (s * 64)) + u * 4096);
    if (first) {
      lds_wait_ge(L, L_FULL, 2 * z + 2);
#pragma unroll
      for (int n = 0; n < 8; ++n)
#pragma unroll
        for (int u = 0; u < 2; ++u) vf[n][u] = trread(S + (va0 ^ (n * 32)) + u * 4096);
      asm volatile("s_waitcnt lgkmcnt(0)" ::: "memory");
      if (lane == 0) *(volatile LAS unsigned*)(L + L_FREE + slot * 4) = z + 1;
      lds_wait_ge(L, L_QRDY, 1);
#pragma unroll
      for (int s = 0; s < 4; ++s) qf[s] = *(LAS v4u*)(L + L_QB + (i & 3) * 256 + s * 64 + grp * 16);
    }
    f32x4 sc[2];
#pragma unroll
    for (int u = 0; u < 2; ++u) {
      sc[u] = f32x4{0.f, 0.f, 0.f, 0.f};
#pragma unroll
      for (int s = 0; s < 4; ++s) sc[u] = mfma(kf[u][s], qf[s], sc[u]);
    }
    float xs[8];
#pragma unroll
    for (int k = 0; k < 4; ++k) {
      const unsigned r0 = cvtpk(sc[k >> 1][(k & 1) * 2], sc[k >> 1][(k & 1) * 2 + 1]);
      const unsigned r1 = cvtpk(lo16(r0) * SCALE, hi16(r0) * SCALE);
      xs[2 * k] = lo16(r1); xs[2 * k + 1] = hi16(r1);
    }
    float tm = fmaxf(fmaxf(fmaxf(xs[0], xs[1]), fmaxf(xs[2], xs[3])), fmaxf(fmaxf(xs[4], xs[5]), fmaxf(xs[6], xs[7])));
    tm = x16_32_max(tm);
    const float mn = fmaxf(m, tm), alpha = __builtin_amdgcn_exp2f((m - mn) * LOG2E);
    m = mn;
    float pe[8], ps = 0.f;
#pragma unroll
    for (int k = 0; k < 8; ++k) { pe[k] = __builtin_amdgcn_exp2f((xs[k] - mn) * LOG2E); ps += pe[k]; }
    l = l * alpha + ps;
    const v4u pf = {cvtpk(pe[0], pe[1]), cvtpk(pe[2], pe[3]), cvtpk(pe[4], pe[5]), cvtpk(pe[6], pe[7])};
#pragma unroll
    for (int n = 0; n < 8; ++n) acc[n] *= alpha;
    if (!first) {
      lds_wait_ge(L, L_FULL, 2 * z + 2);
#pragma unroll
      for (int n = 0; n < 8; ++n)
#pragma unroll
        for (int u = 0; u < 2; ++u) vf[n][u] = trread(S + (va0 ^ (n * 32)) + u * 4096);
      asm volatile("s_waitcnt lgkmcnt(0)" ::: "memory");
      if (lane == 0) *(volatile LAS unsigned*)(L + L_FREE + slot * 4) = z + 1;
    }
#pragma unroll
    for (int n = 0; n < 8; ++n) acc[n] = mfma(v4u{vf[n][0][0], vf[n][0][1], vf[n][1][0], vf[n][1][1]}, pf, acc[n]);
    first = false;
    slot += 4; if (slot >= NS) slot -= NS;
  }
  l = x16_32_sum(l);
  if (i < 4) {
#pragma unroll
    for (int n = 0; n < 8; ++n) *(LAS f32x4*)(L + L_MRG + (cw * 4 + i) * 512 + (16 * n + 4 * grp) * 4) = acc[n];
    if (grp == 0) *(LAS f32x2*)(L + L_ML + (i * 4 + cw) * 8) = f32x2{m, l};
  }
  if (cw == 0) STAMP(5);
  bar();
  {
    const f32x4 ml0 = *(LAS f32x4*)(L + L_ML + cw * 32), ml1 = *(LAS f32x4*)(L + L_ML + cw * 32 + 16);
    const float M = fmaxf(fmaxf(ml0[0], ml0[2]), fmaxf(ml1[0], ml1[2]));
    const float e0 = __builtin_amdgcn_exp2f((ml0[0] - M) * LOG2E), e1 = __builtin_amdgcn_exp2f((ml0[2] - M) * LOG2E),
                e2 = __builtin_amdgcn_exp2f((ml1[0] - M) * LOG2E), e3 = __builtin_amdgcn_exp2f((ml1[2] - M) * LOG2E);
    const f32x2 o0 = *(LAS f32x2*)(L + L_MRG + (0 + cw) * 512 + lane * 8), o1 = *(LAS f32x2*)(L + L_MRG + (4 + cw) * 512 + lane * 8),
                o2 = *(LAS f32x2*)(L + L_MRG + (8 + cw) * 512 + lane * 8), o3 = *(LAS f32x2*)(L + L_MRG + (12 + cw) * 512 + lane * 8);
    const f32x2 o = o0 * e0 + o1 * e1 + o2 * e2 + o3 * e3;
    const float Ls = ml0[1] * e0 + ml0[3] * e1 + ml1[1] * e2 + ml1[3] * e3;
    __builtin_amdgcn_raw_buffer_store_b128(v4u{__float_as_uint(o[0]), tagA, __float_as_uint(o[1]), tagB}, rw, W_O + bb * 4096 + t * 16, 0, 16);
    if (lane == 0) __builtin_amdgcn_raw_buffer_store_b128(v4u{__float_as_uint(M), tagA, __float_as_uint(Ls), tagB}, rw, W_ML + bb * 128 + cw * 16, 0, 16);
  }
  if (cw == 0) STAMP(6);
  v4u go[4], gm[4];
  const int p8 = lane >> 3, k8 = lane & 7;
  const int ao = W_O + (g * 32 + p8) * 4096 + (8 * j + k8) * 16, am = W_ML + (g * 32 + p8) * 128 + (j >> 3) * 16;
  if (cw == 0) {
#pragma unroll
    for (int it = 0; it < 4; ++it) { go[it] = __builtin_amdgcn_raw_buffer_load_b128(rw, ao + it * 8 * 4096, 0, 16); gm[it] = __builtin_amdgcn_raw_buffer_load_b128(rw, am + it * 8 * 128, 0, 16); }
  }
  bar();
  unsigned wr[24];
  {
    LAS char* d1 = L + ((NTILE + 1) % NS) * SLOT;
    LAS char* d2 = L + ((NTILE + 2) % NS) * SLOT;
#pragma unroll
    for (int u = 0; u < 16; ++u) wr[u] = *(LAS unsigned*)(d2 + u * 1024 + t * 4);
#pragma unroll
    for (int u = 0; u < 8; ++u) wr[16 + u] = *(LAS unsigned*)(d1 + 8192 + u * 1024 + t * 4);
    if (cw == 1) {
      f32x4 a0 = {0.f, 0.f, 0.f, 0.f}, a1 = {0.f, 0.f, 0.f, 0.f};
      LAS char* wp = d1 + (8 * grp + (i >> 2)) * 16 + (i & 1) * 8;
#pragma unroll
      for (int s2 = 0; s2 < 16; ++s2) {
        const v2u e0 = trread(wp + s2 * 512), e1 = trread(wp + s2 * 512 + 64);
        const v4u xb = *(LAS v4u*)(L + L_XN + s2 * 64 + grp * 16);
        if (s2 & 1) a1 = mfma(v4u{e0[0], e0[1], e1[0], e1[1]}, xb, a1);
        else a0 = mfma(v4u{e0[0], e0[1], e1[0], e1[1]}, xb, a0);
      }
      a0 += a1;
      f32x4 h;
#pragma unroll
      for (int k = 0; k < 4; ++k) h[k] = r32 < 28 ? fmaxf(rbf(a0[k]), 0.f) : 0.f;
      if (i == 0 && grp < 2) *(LAS f32x4*)(L + L_OV + 64 + grp * 16) = h;
    }
  }
  if (cw == 0) {
    _Pragma("unroll 1") for (unsigned n = 0; n < SPIN_CAP; ++n) {
      bool ok = true;
#pragma unroll
      for (int it = 0; it < 4; ++it) ok = ok && go[it][1] == tagA && go[it][3] == tagB && gm[it][1] == tagA && gm[it][3] == tagB;
      if (__all(ok)) break;
      asm volatile("" ::: "memory");
#pragma unroll
      for (int it = 0; it < 4; ++it) { go[it] = __builtin_amdgcn_raw_buffer_load_b128(rw, ao + it * 8 * 4096, 0, 16); gm[it] = __builtin_amdgcn_raw_buffer_load_b128(rw, am + it * 8 * 128, 0, 16); }
    }
    STAMP(7);
    float M = fmaxf(fmaxf(__uint_as_float(gm[0][0]), __uint_as_float(gm[1][0])), fmaxf(__uint_as_float(gm[2][0]), __uint_as_float(gm[3][0])));
    M = fmaxf(M, dpp<ROR8>(M)); M = x16_32_max(M);
    float Ls = 0.f, o0 = 0.f, o1 = 0.f;
#pragma unroll
    for (int it = 0; it < 4; ++it) {
      const float e = __builtin_amdgcn_exp2f((__uint_as_float(gm[it][0]) - M) * LOG2E);
      Ls += e * __uint_as_float(gm[it][2]); o0 += e * __uint_as_float(go[it][0]); o1 += e * __uint_as_float(go[it][2]);
    }
    Ls += dpp<ROR8>(Ls); o0 += dpp<ROR8>(o0); o1 += dpp<ROR8>(o1);
    Ls = x16_32_sum(Ls); o0 = x16_32_sum(o0); o1 = x16_32_sum(o1);
    if (lane < 8) *(LAS f32x2*)(L + L_OV + lane * 8) = f32x2{rbf(o0 / Ls), rbf(o1 / Ls)};
  }
  bar();
  float y0 = 0.f, y1 = 0.f;
#pragma unroll
  for (int u = 0; u < 6; ++u) {
    const f32x4 c = *(LAS f32x4*)(L + L_OV + u * 16);
#pragma unroll
    for (int k = 0; k < 4; ++k) { y0 = fmaf(c[k], lo16(wr[4 * u + k]), y0); y1 = fmaf(c[k], hi16(wr[4 * u + k]), y1); }
  }
  __builtin_amdgcn_raw_buffer_store_b128(v4u{__float_as_uint(y0), tagA, __float_as_uint(y1), tagB}, rw, W_P + b * 4096 + t * 16, 0, 16);
  if (cw == 0) STAMP(8);
  const int pc = xcd * 32 + r32;
  const unsigned xr = *(const unsigned*)(x + 2 * pc);
  v4u gp;
  _Pragma("unroll 1") for (unsigned n = 0; n < SPIN_CAP; ++n) {
    gp = __builtin_amdgcn_raw_buffer_load_b128(rw, W_P + t * 4096 + pc * 16, 0, 16);
    if (__all(gp[1] == tagA && gp[3] == tagB)) break;
    asm volatile("" ::: "memory");
  }
  y0 = wave_sum(__uint_as_float(gp[0])); y1 = wave_sum(__uint_as_float(gp[2]));
  if (lane == 0) *(LAS f32x2*)(L + L_RED + cw * 8) = f32x2{y0, y1};
  bar();
  if (t == 0) {
    const f32x4 s0 = *(LAS f32x4*)(L + L_RED), s1 = *(LAS f32x4*)(L + L_RED + 16);
    *(unsigned*)(out + 2 * pc) = cvtpk(lo16(xr) + ((s0[0] + s0[2]) + (s1[0] + s1[2])), hi16(xr) + ((s0[1] + s0[3]) + (s1[1] + s1[3])));
    if (b == 0) __builtin_amdgcn_raw_buffer_store_b64(v2u{tagA, tagB}, rw, W_E, 0, 16);
  }
  if (cw == 0) STAMP(9);
}

extern "C" void kernel_launch(void* const* d_in, const int* in_sizes, int n_in, void* d_out, int out_size, void* d_ws, size_t ws_size, hipStream_t stream) {
  (void)hipFuncSetAttribute((const void*)fused_decode, hipFuncAttributeMaxDynamicSharedMemorySize, L_END);
  fused_decode<<<NWG, NTHR, L_END, stream>>>((const u16*)d_in[0], (const u16*)d_in[1], (const u16*)d_in[2], (const u16*)d_in[3], (const u16*)d_in[4],
                                             (const u16*)d_in[6], (const u16*)d_in[7], (u16*)d_out, (char*)d_ws);
}
```

```cpp
#include <hip/hip_runtime.h>
#include <stdint.h>

#define DBG 0
#define WIN 24
#define AUXW 2

#define LAS __attribute__((address_space(3)))
typedef unsigned v4u __attribute__((ext_vector_type(4)));
typedef unsigned v2u __attribute__((ext_vector_type(2)));
typedef float f32x4 __attribute__((ext_vector_type(4)));
typedef float f32x2 __attribute__((ext_vector_type(2)));
typedef __bf16 bf16x8 __attribute__((ext_vector_type(8)));
typedef __bf16 bf16x2 __attribute__((ext_vector_type(2)));
typedef short s16x4 __attribute__((ext_vector_type(4)));
typedef unsigned short u16;

constexpr int D = 512, HQ = 32, HKV = 8, SK = 65536, DH = 128, DFF = 1792;
constexpr int NWG = 256, NTHR = 320, NTILE = 64;
constexpr int NS = 9, SLOT = 16384;
constexpr int L_XN = NS * SLOT, L_QB = L_XN + 1024, L_MRG = L_QB + 1024, L_ML = L_MRG + 8192, L_OV = L_ML + 128, L_RED = L_OV + 128,
              L_FULL = L_RED + 64, L_FREE = L_FULL + 256, L_QRDY = L_FREE + 64, L_END = L_QRDY + 64;
static_assert(L_END <= 163840, "LDS");
constexpr int W_E = 0, W_Q = 4096, W_ML = 32768, W_O = 65536, W_P = W_O + (1 << 20), W_END = W_P + (1 << 20), W_DBG = 4 << 20;
constexpr unsigned SPIN_CAP = 1u << 16;

__device__ __forceinline__ unsigned cvtpk(float lo, float hi) { return __builtin_bit_cast(unsigned, __builtin_convertvector(f32x2{lo, hi}, bf16x2)); }
__device__ __forceinline__ float lo16(unsigned u) { return __uint_as_float(u << 16); }
__device__ __forceinline__ float hi16(unsigned u) { return __uint_as_float(u & 0xffff0000u); }
__device__ __forceinline__ float rbf(float x) { return lo16(cvtpk(x, x)); }
template <int N> __device__ __forceinline__ void wait_vm() { asm volatile("s_waitcnt vmcnt(%0)" ::"n"(N) : "memory"); }
template <int CTRL> __device__ __forceinline__ float dpp(float x) { return __builtin_bit_cast(float, __builtin_amdgcn_mov_dpp(__builtin_bit_cast(int, x), CTRL, 0xf, 0xf, true)); }
constexpr int XOR1 = 0xB1, XOR2 = 0x4E, XOR7 = 0x141, ROR8 = 0x128, ROR4 = 0x124;
__device__ __forceinline__ float x16_32_max(float x) {
  auto s = __builtin_amdgcn_permlane16_swap(__float_as_uint(x), __float_as_uint(x), false, false);
  x = fmaxf(__uint_as_float(s[0]), __uint_as_float(s[1]));
  auto t = __builtin_amdgcn_permlane32_swap(__float_as_uint(x), __float_as_uint(x), false, false);
  return fmaxf(__uint_as_float(t[0]), __uint_as_float(t[1]));
}
__device__ __forceinline__ float x16_32_sum(float x) {
  auto s = __builtin_amdgcn_permlane16_swap(__float_as_uint(x), __float_as_uint(x), false, false);
  x = __uint_as_float(s[0]) + __uint_as_float(s[1]);
  auto t = __builtin_amdgcn_permlane32_swap(__float_as_uint(x), __float_as_uint(x), false, false);
  return __uint_as_float(t[0]) + __uint_as_float(t[1]);
}
__device__ __forceinline__ float wave_sum(float x) {
  x += dpp<XOR1>(x); x += dpp<XOR2>(x); x += dpp<XOR7>(x); x += dpp<ROR8>(x);
  return x16_32_sum(x);
}
__device__ __forceinline__ f32x4 mfma(v4u a, v4u b, f32x4 c) { return __builtin_amdgcn_mfma_f32_16x16x32_bf16(__builtin_bit_cast(bf16x8, a), __builtin_bit_cast(bf16x8, b), c, 0, 0, 0); }
__device__ __forceinline__ v2u trread(LAS char* p) { return __builtin_bit_cast(v2u, __builtin_amdgcn_ds_read_tr16_b64_v4i16((LAS s16x4*)p)); }
__device__ __forceinline__ void bar() { asm volatile("s_waitcnt lgkmcnt(0)\n\ts_barrier" ::: "memory"); }
__device__ __forceinline__ void lds_wait_ge(LAS char* L, int off, unsigned need) {
  volatile LAS unsigned* p = (volatile LAS unsigned*)(L + off);
  _Pragma("unroll 1") for (unsigned n = 0; n < SPIN_CAP; ++n) {
    if (__builtin_amdgcn_readfirstlane(*p) >= need) break;
    __builtin_amdgcn_s_sleep(1);
  }
  asm volatile("" ::: "memory");
}
#define STAMP(k) do {} while (0)

__global__ void __launch_bounds__(NTHR) fused_decode(const u16* __restrict__ x, const u16* __restrict__ Kc, const u16* __restrict__ Vc, const u16* __restrict__ wq,
                                                     const u16* __restrict__ wo, const u16* __restrict__ w1, const u16* __restrict__ w2, u16* __restrict__ out, char* ws) {
  extern __shared__ __attribute__((aligned(16))) char lds_raw[];
  LAS char* L = (LAS char*)lds_raw;
  const int b = blockIdx.x, lane = threadIdx.x & 63, wave = __builtin_amdgcn_readfirstlane(threadIdx.x >> 6);
  const int xcd = b & 7, r32 = b >> 3;
  const int g = xcd, j = r32;
  const int bb = g * 32 + j;

  if (wave == 0) {
    __builtin_amdgcn_s_setprio(3);
    auto rk = __builtin_amdgcn_make_buffer_rsrc((void*)Kc, 0, HKV * SK * DH * 2, 0x00020000);
    auto rv = __builtin_amdgcn_make_buffer_rsrc((void*)Vc, 0, HKV * SK * DH * 2, 0x00020000);
    auto rq = __builtin_amdgcn_make_buffer_rsrc((void*)wq, 0, D * HQ * DH * 2, 0x00020000);
    {
      const int vo = (lane >> 1) * (HQ * DH * 2) + (lane & 1) * 16, so = bb * 32;
#pragma unroll
      for (int u = 0; u < 16; ++u) __builtin_amdgcn_raw_ptr_buffer_load_lds(rq, L + u * 1024, 16, vo, so + u * 32 * (HQ * DH * 2), 0, AUXW);
    }
    const unsigned fa = L_FULL + lane * 4;
    asm volatile("ds_write_b32 %0, %1\n\tds_write_b32 %0, %1 offset:128\n\ts_waitcnt lgkmcnt(0)\n\ts_barrier" ::"v"(fa), "v"(0u) : "memory");
    const int rr = lane >> 4, p = lane & 15;
    int vk[4], vv[2];
#pragma unroll
    for (int u = 0; u < 4; ++u) vk[u] = rr * 256 + ((p ^ (4 * u + rr)) << 4);
#pragma unroll
    for (int u = 0; u < 2; ++u) vv[u] = rr * 256 + ((p ^ (2 * (4 * u + rr))) << 4);
    int slot = 1;
    for (int z = 1; z <= NTILE; ++z) {
      const int so = (bb * NTILE + z - 1) * 8192;
      LAS char* dst = L + slot * SLOT;
      const int nslot = (slot + 1 == NS) ? 0 : slot + 1;
      unsigned fr;
#pragma unroll
      for (int u = 0; u < 16; ++u) {
        wait_vm<WIN - 1>();
        if (((u - WIN + 1) & 7) == 0) {
          const int v = (16 * z + u - WIN + 1) / 8;
          asm volatile("ds_write_b32 %0, %1" ::"v"(fa), "v"(v < 0 ? 0 : v) : "memory");
        }
        if (u < 8) __builtin_amdgcn_raw_ptr_buffer_load_lds(rk, dst + u * 1024, 16, vk[u & 3], so + u * 1024, 0, 2);
        else __builtin_amdgcn_raw_ptr_buffer_load_lds(rv, dst + u * 1024, 16, vv[u & 1], so + (u - 8) * 1024, 0, 2);
        if (u == 13) asm volatile("ds_read_b32 %0, %1" : "=v"(fr) : "v"(L_FREE + nslot * 4) : "memory");
      }
      asm volatile("s_waitcnt lgkmcnt(0)" : "+v"(fr)::"memory");
      if (z + 1 >= NS) {
        const unsigned need = z + 2 - NS;
        _Pragma("unroll 1") for (unsigned n = 0; n < SPIN_CAP && (unsigned)__builtin_amdgcn_readfirstlane(fr) < need; ++n)
          asm volatile("s_sleep 1\n\tds_read_b32 %0, %1\n\ts_waitcnt lgkmcnt(0)" : "=v"(fr) : "v"(L_FREE + nslot * 4) : "memory");
      }
      slot = nslot;
    }
    {
      auto r1 = __builtin_amdgcn_make_buffer_rsrc((void*)w1, 0, D * DFF * 2, 0x00020000);
      auto r2 = __builtin_amdgcn_make_buffer_rsrc((void*)w2, 0, D * DFF * 2, 0x00020000);
      auto ro = __builtin_amdgcn_make_buffer_rsrc((void*)wo, 0, D * HQ * DH * 2, 0x00020000);
      const int f0 = (xcd * 28 + (r32 < 28 ? r32 : 27)) * 8;
      LAS char* d1 = L + ((NTILE + 1) % NS) * SLOT;
      LAS char* d2 = L + ((NTILE + 2) % NS) * SLOT;
      constexpr int W2 = (WIN + 32 <= 63) ? 63 : WIN;
      constexpr int I0 = 16 * (NTILE + 1);
      constexpr int PUB0 = (I0 - 1 - WIN + 1) / 8;
#pragma unroll
      for (int u = 0; u < 32; ++u) {
        if (W2 != 63) {
          wait_vm<W2 - 1>();
          if (((I0 + u - W2 + 1) & 7) == 0 && (I0 + u - W2 + 1) / 8 <= 2 * NTILE + 2) asm volatile("ds_write_b32 %0, %1" ::"v"(fa), "v"((I0 + u - W2 + 1) / 8) : "memory");
        }
        if (u == 16) {
          unsigned fr = 0;
          _Pragma("unroll 1") for (unsigned n = 0; n < SPIN_CAP && (unsigned)__builtin_amdgcn_readfirstlane(fr) < NTILE + 3 - NS; ++n)
            asm volatile("ds_read_b32 %0, %1\n\ts_waitcnt lgkmcnt(0)" : "=v"(fr) : "v"(L_FREE + ((NTILE + 2) % NS) * 4) : "memory");
        }
        if (u < 8) __builtin_amdgcn_raw_ptr_buffer_load_lds(r1, d1 + u * 1024, 16, lane * (DFF * 2), f0 * 2 + u * 64 * (DFF * 2), 0, AUXW);
        else if (u < 16) __builtin_amdgcn_raw_ptr_buffer_load_lds(r2, d1 + u * 1024, 16, lane * 16, (f0 + u - 8) * 1024, 0, 2);
        else __builtin_amdgcn_raw_ptr_buffer_load_lds(ro, d2 + (u - 16) * 1024, 16, lane * 16, (bb * 16 + u - 16) * 1024, 0, 2);
      }
      if (2 * NTILE - 1 > PUB0) { wait_vm<32 + 24>(); asm volatile("ds_write_b32 %0, %1" ::"v"(fa), "v"(2 * NTILE - 1) : "memory"); }
      if (2 * NTILE > PUB0) { wait_vm<32 + 16>(); asm volatile("ds_write_b32 %0, %1" ::"v"(fa), "v"(2 * NTILE) : "memory"); }
      wait_vm<32 + 8>(); asm volatile("ds_write_b32 %0, %1" ::"v"(fa), "v"(2 * NTILE + 1) : "memory");
      wait_vm<32>(); asm volatile("ds_write_b32 %0, %1" ::"v"(fa), "v"(2 * NTILE + 2) : "memory");
    }
    STAMP(1);
    bar();
    wait_vm<0>();
    bar();
    return;
  }

  const int cw = wave - 1, t = threadIdx.x - 64, grp = lane >> 4, i = lane & 15;
  if (cw == 0) STAMP(0);
  auto rw = __builtin_amdgcn_make_buffer_rsrc((void*)ws, 0, W_END, 0x00020000);
  const v2u E = __builtin_amdgcn_raw_buffer_load_b64(rw, W_E, 0, 16);
  v4u xv;
  if (cw == 0) xv = *(const v4u*)(x + lane * 8);
  bar();
  const unsigned tagA = E[0] + 1u, tagB = E[1] + 1u;

  if (cw == 0) {
    float xf[8];
#pragma unroll
    for (int k = 0; k < 4; ++k) { xf[2 * k] = lo16(xv[k]); xf[2 * k + 1] = hi16(xv[k]); }
    float s = 0.f;
#pragma unroll
    for (int k = 0; k < 8; ++k) s += xf[k];
    const float mean = wave_sum(s) * (1.f / D);
    float v = 0.f;
#pragma unroll
    for (int k = 0; k < 8; ++k) { const float d = xf[k] - mean; v += d * d; }
    const float var = wave_sum(v) * (1.f / D);
    const float mb = rbf(mean), den = rbf(sqrtf(rbf(rbf(var) + 1e-5f)));
    v4u xn;
#pragma unroll
    for (int k = 0; k < 4; ++k) xn[k] = cvtpk(rbf(xf[2 * k] - mb) / den, rbf(xf[2 * k + 1] - mb) / den);
    *(LAS v4u*)(L + L_XN + lane * 16) = xn;
    lds_wait_ge(L, L_FULL, 2);
    STAMP(2);
    f32x4 a0 = {0.f, 0.f, 0.f, 0.f}, a1 = {0.f, 0.f, 0.f, 0.f};
    LAS char* wp = L + (8 * grp + (i >> 2)) * 32 + (i & 3) * 8;
#pragma unroll
    for (int s2 = 0; s2 < 16; ++s2) {
      const v2u e0 = trread(wp + s2 * 1024), e1 = trread(wp + s2 * 1024 + 128);
      const v4u xb = *(LAS v4u*)(L + L_XN + s2 * 64 + grp * 16);
      if (s2 & 1) a1 = mfma(v4u{e0[0], e0[1], e1[0], e1[1]}, xb, a1);
      else a0 = mfma(v4u{e0[0], e0[1], e1[0], e1[1]}, xb, a0);
    }
    a0 += a1;
    if (i == 0) __builtin_amdgcn_raw_buffer_store_b128(v4u{cvtpk(a0[0], a0[1]), tagA, cvtpk(a0[2], a0[3]), tagB}, rw, W_Q + bb * 64 + grp * 16, 0, 16);
    asm volatile("" ::: "memory");
    if (lane == 0) *(volatile LAS unsigned*)(L + L_FREE) = 1u;
    STAMP(3);
    v4u q0, q1;
    _Pragma("unroll 1") for (unsigned n = 0; n < SPIN_CAP; ++n) {
      q0 = __builtin_amdgcn_raw_buffer_load_b128(rw, W_Q + g * 2048 + lane * 32, 0, 16);
      q1 = __builtin_amdgcn_raw_buffer_load_b128(rw, W_Q + g * 2048 + lane * 32 + 16, 0, 16);
      const bool ok = q0[1] == tagA && q0[3] == tagB && q1[1] == tagA && q1[3] == tagB;
      if (__all(ok)) break;
      asm volatile("s_sleep 1" ::: "memory");
    }
    *(LAS v4u*)(L + L_QB + lane * 16) = v4u{q0[0], q0[2], q1[0], q1[2]};
    asm volatile("s_waitcnt lgkmcnt(0)" ::: "memory");
    if (lane == 0) *(volatile LAS unsigned*)(L + L_QRDY) = 1u;
    STAMP(4);
  }

  v4u qf[4];
  f32x4 acc[8];
#pragma unroll
  for (int n = 0; n < 8; ++n) acc[n] = f32x4{0.f, 0.f, 0.f, 0.f};
  float m = -INFINITY, l = 0.f;
  const int ka0 = i * 256 + ((grp ^ i) << 4);
  const int va0 = 8192 + (4 * grp + (i >> 2)) * 256 + ((((i & 3) >> 1) ^ (2 * (4 * (grp & 1) + (i >> 2)))) << 4) + (i & 1) * 8;
  constexpr float LOG2E = 1.4426950408889634f, SCALE = 0.08838834764831845f;
  int slot = (cw == 0 ? 4 : cw);
  bool first = true;
  for (int z = slot; z <= NTILE; z += 4) {
    LAS char* S = L + slot * SLOT;
    v4u kf[2][4];
    v2u vf[8][2];
    lds_wait_ge(L, L_FULL, 2 * z + 1);
#pragma unroll
    for (int s = 0; s < 4; ++s)
#pragma unroll
      for (int u = 0; u < 2; ++u) kf[u][s] = *(LAS v4u*)(S + (ka0 ^ (s * 64)) + u * 4096);
    if (first) {
      lds_wait_ge(L, L_FULL, 2 * z + 2);
#pragma unroll
      for (int n = 0; n < 8; ++n)
#pragma unroll
        for (int u = 0; u < 2; ++u) vf[n][u] = trread(S + (va0 ^ (n * 32)) + u * 4096);
      asm volatile("s_waitcnt lgkmcnt(0)" ::: "memory");
      if (lane == 0) *(volatile LAS unsigned*)(L + L_FREE + slot * 4) = z + 1;
      lds_wait_ge(L, L_QRDY, 1);
#pragma unroll
      for (int s = 0; s < 4; ++s) qf[s] = *(LAS v4u*)(L + L_QB + (i & 3) * 256 + s * 64 + grp * 16);
    }
    f32x4 sc[2];
#pragma unroll
    for (int u = 0; u < 2; ++u) {
      sc[u] = f32x4{0.f, 0.f, 0.f, 0.f};
#pragma unroll
      for (int s = 0; s < 4; ++s) sc[u] = mfma(kf[u][s], qf[s], sc[u]);
    }
    float xs[8];
#pragma unroll
    for (int k = 0; k < 4; ++k) {
      const unsigned r0 = cvtpk(sc[k >> 1][(k & 1) * 2], sc[k >> 1][(k & 1) * 2 + 1]);
      const unsigned r1 = cvtpk(lo16(r0) * SCALE, hi16(r0) * SCALE);
      xs[2 * k] = lo16(r1); xs[2 * k + 1] = hi16(r1);
    }
    float tm = fmaxf(fmaxf(fmaxf(xs[0], xs[1]), fmaxf(xs[2], xs[3])), fmaxf(fmaxf(xs[4], xs[5]), fmaxf(xs[6], xs[7])));
    tm = x16_32_max(tm);
    const float mn = fmaxf(m, tm), alpha = __builtin_amdgcn_exp2f((m - mn) * LOG2E);
    m = mn;
    float pe[8], ps = 0.f;
#pragma unroll
    for (int k = 0; k < 8; ++k) { pe[k] = __builtin_amdgcn_exp2f((xs[k] - mn) * LOG2E); ps += pe[k]; }
    l = l * alpha + ps;
    const v4u pf = {cvtpk(pe[0], pe[1]), cvtpk(pe[2], pe[3]), cvtpk(pe[4], pe[5]), cvtpk(pe[6], pe[7])};
#pragma unroll
    for (int n = 0; n < 8; ++n) acc[n] *= alpha;
    if (!first) {
      lds_wait_ge(L, L_FULL, 2 * z + 2);
#pragma unroll
      for (int n = 0; n < 8; ++n)
#pragma unroll
        for (int u = 0; u < 2; ++u) vf[n][u] = trread(S + (va0 ^ (n * 32)) + u * 4096);
      asm volatile("s_waitcnt lgkmcnt(0)" ::: "memory");
      if (lane == 0) *(volatile LAS unsigned*)(L + L_FREE + slot * 4) = z + 1;
    }
#pragma unroll
    for (int n = 0; n < 8; ++n) acc[n] = mfma(v4u{vf[n][0][0], vf[n][0][1], vf[n][1][0], vf[n][1][1]}, pf, acc[n]);
    first = false;
    slot += 4; if (slot >= NS) slot -= NS;
  }
  l = x16_32_sum(l);
  if (i < 4) {
#pragma unroll
    for (int n = 0; n < 8; ++n) *(LAS f32x4*)(L + L_MRG + (cw * 4 + i) * 512 + (16 * n + 4 * grp) * 4) = acc[n];
    if (grp == 0) *(LAS f32x2*)(L + L_ML + (i * 4 + cw) * 8) = f32x2{m, l};
  }
  if (cw == 0) STAMP(5);
  bar();
  {
    const f32x4 ml0 = *(LAS f32x4*)(L + L_ML + cw * 32), ml1 = *(LAS f32x4*)(L + L_ML + cw * 32 + 16);
    const float M = fmaxf(fmaxf(ml0[0], ml0[2]), fmaxf(ml1[0], ml1[2]));
    const float e0 = __builtin_amdgcn_exp2f((ml0[0] - M) * LOG2E), e1 = __builtin_amdgcn_exp2f((ml0[2] - M) * LOG2E),
                e2 = __builtin_amdgcn_exp2f((ml1[0] - M) * LOG2E), e3 = __builtin_amdgcn_exp2f((ml1[2] - M) * LOG2E);
    const f32x2 o0 = *(LAS f32x2*)(L + L_MRG + (0 + cw) * 512 + lane * 8), o1 = *(LAS f32x2*)(L + L_MRG + (4 + cw) * 512 + lane * 8),
                o2 = *(LAS f32x2*)(L + L_MRG + (8 + cw) * 512 + lane * 8), o3 = *(LAS f32x2*)(L + L_MRG + (12 + cw) * 512 + lane * 8);
    const f32x2 o = o0 * e0 + o1 * e1 + o2 * e2 + o3 * e3;
    const float Ls = ml0[1] * e0 + ml0[3] * e1 + ml1[1] * e2 + ml1[3] * e3;
    __builtin_amdgcn_raw_buffer_store_b128(v4u{__float_as_uint(o[0]), tagA, __float_as_uint(o[1]), tagB}, rw, W_O + bb * 4096 + t * 16, 0, 16);
    if (lane == 0) __builtin_amdgcn_raw_buffer_store_b128(v4u{__float_as_uint(M), tagA, __float_as_uint(Ls), tagB}, rw, W_ML + bb * 128 + cw * 16, 0, 16);
  }
  if (cw == 0) STAMP(6);
  v4u go[4], gm[4];
  const int p8 = lane >> 3, k8 = lane & 7;
  const int ao = W_O + (g * 32 + p8) * 4096 + (8 * j + k8) * 16, am = W_ML + (g * 32 + p8) * 128 + (j >> 3) * 16;
  if (cw == 0) {
#pragma unroll
    for (int it = 0; it < 4; ++it) { go[it] = __builtin_amdgcn_raw_buffer_load_b128(rw, ao + it * 8 * 4096, 0, 16); gm[it] = __builtin_amdgcn_raw_buffer_load_b128(rw, am + it * 8 * 128, 0, 16); }
  }
  bar();
  unsigned wr[24];
  {
    LAS char* d1 = L + ((NTILE + 1) % NS) * SLOT;
    LAS char* d2 = L + ((NTILE + 2) % NS) * SLOT;
#pragma unroll
    for (int u = 0; u < 16; ++u) wr[u] = *(LAS unsigned*)(d2 + u * 1024 + t * 4);
#pragma unroll
    for (int u = 0; u < 8; ++u) wr[16 + u] = *(LAS unsigned*)(d1 + 8192 + u * 1024 + t * 4);
    if (cw == 1) {
      f32x4 a0 = {0.f, 0.f, 0.f, 0.f}, a1 = {0.f, 0.f, 0.f, 0.f};
      LAS char* wp = d1 + (8 * grp + (i >> 2)) * 16 + (i & 1) * 8;
#pragma unroll
      for (int s2 = 0; s2 < 16; ++s2) {
        const v2u e0 = trread(wp + s2 * 512), e1 = trread(wp + s2 * 512 + 64);
        const v4u xb = *(LAS v4u*)(L + L_XN + s2 * 64 + grp * 16);
        if (s2 & 1) a1 = mfma(v4u{e0[0], e0[1], e1[0], e1[1]}, xb, a1);
        else a0 = mfma(v4u{e0[0], e0[1], e1[0], e1[1]}, xb, a0);
      }
      a0 += a1;
      f32x4 h;
#pragma unroll
      for (int k = 0; k < 4; ++k) h[k] = r32 < 28 ? fmaxf(rbf(a0[k]), 0.f) : 0.f;
      if (i == 0 && grp < 2) *(LAS f32x4*)(L + L_OV + 64 + grp * 16) = h;
    }
  }
  if (cw == 0) {
    _Pragma("unroll 1") for (unsigned n = 0; n < SPIN_CAP; ++n) {
      bool ok = true;
#pragma unroll
      for (int it = 0; it < 4; ++it) ok = ok && go[it][1] == tagA && go[it][3] == tagB && gm[it][1] == tagA && gm[it][3] == tagB;
      if (__all(ok)) break;
      asm volatile("" ::: "memory");
#pragma unroll
      for (int it = 0; it < 4; ++it) { go[it] = __builtin_amdgcn_raw_buffer_load_b128(rw, ao + it * 8 * 4096, 0, 16); gm[it] = __builtin_amdgcn_raw_buffer_load_b128(rw, am + it * 8 * 128, 0, 16); }
    }
    STAMP(7);
    float M = fmaxf(fmaxf(__uint_as_float(gm[0][0]), __uint_as_float(gm[1][0])), fmaxf(__uint_as_float(gm[2][0]), __uint_as_float(gm[3][0])));
    M = fmaxf(M, dpp<ROR8>(M)); M = x16_32_max(M);
    float Ls = 0.f, o0 = 0.f, o1 = 0.f;
#pragma unroll
    for (int it = 0; it < 4; ++it) {
      const float e = __builtin_amdgcn_exp2f((__uint_as_float(gm[it][0]) - M) * LOG2E);
      Ls += e * __uint_as_float(gm[it][2]); o0 += e * __uint_as_float(go[it][0]); o1 += e * __uint_as_float(go[it][2]);
    }
    Ls += dpp<ROR8>(Ls); o0 += dpp<ROR8>(o0); o1 += dpp<ROR8>(o1);
    Ls = x16_32_sum(Ls); o0 = x16_32_sum(o0); o1 = x16_32_sum(o1);
    if (lane < 8) *(LAS f32x2*)(L + L_OV + lane * 8) = f32x2{rbf(o0 / Ls), rbf(o1 / Ls)};
  }
  bar();
  float y0 = 0.f, y1 = 0.f;
#pragma unroll
  for (int u = 0; u < 6; ++u) {
    const f32x4 c = *(LAS f32x4*)(L + L_OV + u * 16);
#pragma unroll
    for (int k = 0; k < 4; ++k) { y0 = fmaf(c[k], lo16(wr[4 * u + k]), y0); y1 = fmaf(c[k], hi16(wr[4 * u + k]), y1); }
  }
  __builtin_amdgcn_raw_buffer_store_b128(v4u{__float_as_uint(y0), tagA, __float_as_uint(y1), tagB}, rw, W_P + b * 4096 + t * 16, 0, 16);
  if (cw == 0) STAMP(8);
  const int pc = xcd * 32 + r32;
  const unsigned xr = *(const unsigned*)(x + 2 * pc);
  v4u gp;
  _Pragma("unroll 1") for (unsigned n = 0; n < SPIN_CAP; ++n) {
    gp = __builtin_amdgcn_raw_buffer_load_b128(rw, W_P + t * 4096 + pc * 16, 0, 16);
    if (__all(gp[1] == tagA && gp[3] == tagB)) break;
    asm volatile("" ::: "memory");
  }
  y0 = wave_sum(__uint_as_float(gp[0])); y1 = wave_sum(__uint_as_float(gp[2]));
  if (lane == 0) *(LAS f32x2*)(L + L_RED + cw * 8) = f32x2{y0, y1};
  bar();
  if (t == 0) {
    const f32x4 s0 = *(LAS f32x4*)(L + L_RED), s1 = *(LAS f32x4*)(L + L_RED + 16);
    *(unsigned*)(out + 2 * pc) = cvtpk(lo16(xr) + ((s0[0] + s0[2]) + (s1[0] + s1[2])), hi16(xr) + ((s0[1] + s0[3]) + (s1[1] + s1[3])));
    if (b == 0) __builtin_amdgcn_raw_buffer_store_b64(v2u{tagA, tagB}, rw, W_E, 0, 16);
  }
  if (cw == 0) STAMP(9);
}

extern "C" void kernel_launch(void* const* d_in, const int* in_sizes, int n_in, void* d_out, int out_size, void* d_ws, size_t ws_size, hipStream_t stream) {
  (void)hipFuncSetAttribute((const void*)fused_decode, hipFuncAttributeMaxDynamicSharedMemorySize, L_END);
  fused_decode<<<NWG, NTHR, L_END, stream>>>((const u16*)d_in[0], (const u16*)d_in[1], (const u16*)d_in[2], (const u16*)d_in[3], (const u16*)d_in[4],
                                             (const u16*)d_in[6], (const u16*)d_in[7], (u16*)d_out, (char*)d_ws);
}
```

```cpp
#define PROBE 0
#define PMUL 1
#include <hip/hip_runtime.h>
#include <stdint.h>

#define DBG 0
#define WIN 20
#define AUXW 2

#define LAS __attribute__((address_space(3)))
typedef unsigned v4u __attribute__((ext_vector_type(4)));
typedef unsigned v2u __attribute__((ext_vector_type(2)));
typedef float f32x4 __attribute__((ext_vector_type(4)));
typedef float f32x2 __attribute__((ext_vector_type(2)));
typedef __bf16 bf16x8 __attribute__((ext_vector_type(8)));
typedef __bf16 bf16x2 __attribute__((ext_vector_type(2)));
typedef short s16x4 __attribute__((ext_vector_type(4)));
typedef unsigned short u16;

constexpr int D = 512, HQ = 32, HKV = 8, SK = 65536, DH = 128, DFF = 1792;
constexpr int NWG = 256, NTHR = 320, NTILE = 64;
constexpr int NS = 9, SLOT = 16384;
constexpr int L_XN = NS * SLOT, L_QB = L_XN + 1024, L_MRG = L_QB + 1024, L_ML = L_MRG + 8192, L_OV = L_ML + 128, L_RED = L_OV + 128,
              L_FULL = L_RED + 64, L_FREE = L_FULL + 256, L_QRDY = L_FREE + 64, L_END = L_QRDY + 64;
static_assert(L_END <= 163840, "LDS");
constexpr int W_E = 0, W_Q = 4096, W_ML = 32768, W_O = 65536, W_P = W_O + (1 << 20), W_END = W_P + (1 << 20), W_DBG = 4 << 20;
constexpr unsigned SPIN_CAP = 1u << 16;

__device__ __forceinline__ unsigned cvtpk(float lo, float hi) { return __builtin_bit_cast(unsigned, __builtin_convertvector(f32x2{lo, hi}, bf16x2)); }
__device__ __forceinline__ float lo16(unsigned u) { return __uint_as_float(u << 16); }
__device__ __forceinline__ float hi16(unsigned u) { return __uint_as_float(u & 0xffff0000u); }
__device__ __forceinline__ float rbf(float x) { return lo16(cvtpk(x, x)); }
template <int N> __device__ __forceinline__ void wait_vm() { asm volatile("s_waitcnt vmcnt(%0)" ::"n"(N) : "memory"); }
template <int CTRL> __device__ __forceinline__ float dpp(float x) { return __builtin_bit_cast(float, __builtin_amdgcn_mov_dpp(__builtin_bit_cast(int, x), CTRL, 0xf, 0xf, true)); }
constexpr int XOR1 = 0xB1, XOR2 = 0x4E, XOR7 = 0x141, ROR8 = 0x128, ROR4 = 0x124;
__device__ __forceinline__ float x16_32_max(float x) {
  auto s = __builtin_amdgcn_permlane16_swap(__float_as_uint(x), __float_as_uint(x), false, false);
  x = fmaxf(__uint_as_float(s[0]), __uint_as_float(s[1]));
  auto t = __builtin_amdgcn_permlane32_swap(__float_as_uint(x), __float_as_uint(x), false, false);
  return fmaxf(__uint_as_float(t[0]), __uint_as_float(t[1]));
}
__device__ __forceinline__ float x16_32_sum(float x) {
  auto s = __builtin_amdgcn_permlane16_swap(__float_as_uint(x), __float_as_uint(x), false, false);
  x = __uint_as_float(s[0]) + __uint_as_float(s[1]);
  auto t = __builtin_amdgcn_permlane32_swap(__float_as_uint(x), __float_as_uint(x), false, false);
  return __uint_as_float(t[0]) + __uint_as_float(t[1]);
}
__device__ __forceinline__ float wave_sum(float x) {
  x += dpp<XOR1>(x); x += dpp<XOR2>(x); x += dpp<XOR7>(x); x += dpp<ROR8>(x);
  return x16_32_sum(x);
}
__device__ __forceinline__ f32x4 mfma(v4u a, v4u b, f32x4 c) { return __builtin_amdgcn_mfma_f32_16x16x32_bf16(__builtin_bit_cast(bf16x8, a), __builtin_bit_cast(bf16x8, b), c, 0, 0, 0); }
__device__ __forceinline__ v2u trread(LAS char* p) { return __builtin_bit_cast(v2u, __builtin_amdgcn_ds_read_tr16_b64_v4i16((LAS s16x4*)p)); }
__device__ __forceinline__ void bar() { asm volatile("s_waitcnt lgkmcnt(0)\n\ts_barrier" ::: "memory"); }
__device__ __forceinline__ void lds_wait_ge(LAS char* L, int off, unsigned need) {
  volatile LAS unsigned* p = (volatile LAS unsigned*)(L + off);
  _Pragma("unroll 1") for (unsigned n = 0; n < SPIN_CAP; ++n) {
    if (__builtin_amdgcn_readfirstlane(*p) >= need) break;
    __builtin_amdgcn_s_sleep(1);
  }
  asm volatile("" ::: "memory");
}
#define STAMP(k) do { if ((k) == 0) pr_t0 = __builtin_amdgcn_s_memrealtime(); if ((k) == PROBE) pr_tk = __builtin_amdgcn_s_memrealtime(); \
    if ((k) == 9) { const unsigned long long tgt = pr_t0 + 6000 + PMUL * (pr_tk - pr_t0); _Pragma("unroll 1") for (int n_ = 0; n_ < 4000000 && __builtin_amdgcn_s_memrealtime() < tgt; ++n_) __builtin_amdgcn_s_sleep(1); } } while (0)

__global__ void __launch_bounds__(NTHR) fused_decode(const u16* __restrict__ x, const u16* __restrict__ Kc, const u16* __restrict__ Vc, const u16* __restrict__ wq,
                                                     const u16* __restrict__ wo, const u16* __restrict__ w1, const u16* __restrict__ w2, u16* __restrict__ out, char* ws) {
  extern __shared__ __attribute__((aligned(16))) char lds_raw[];
  LAS char* L = (LAS char*)lds_raw;
  const int b = blockIdx.x, lane = threadIdx.x & 63, wave = __builtin_amdgcn_readfirstlane(threadIdx.x >> 6);
  const int xcd = b & 7, r32 = b >> 3;
  const int g = xcd, j = r32;
  const int bb = g * 32 + j;
  unsigned long long pr_t0 = 0, pr_tk = 0;

  if (wave == 0) {
    __builtin_amdgcn_s_setprio(3);
    auto rk = __builtin_amdgcn_make_buffer_rsrc((void*)Kc, 0, HKV * SK * DH * 2, 0x00020000);
    auto rv = __builtin_amdgcn_make_buffer_rsrc((void*)Vc, 0, HKV * SK * DH * 2, 0x00020000);
    auto rq = __builtin_amdgcn_make_buffer_rsrc((void*)wq, 0, D * HQ * DH * 2, 0x00020000);
    {
      const int vo = (lane >> 1) * (HQ * DH * 2) + (lane & 1) * 16, so = bb * 32;
#pragma unroll
      for (int u = 0; u < 16; ++u) __builtin_amdgcn_raw_ptr_buffer_load_lds(rq, L + u * 1024, 16, vo, so + u * 32 * (HQ * DH * 2), 0, AUXW);
    }
    const unsigned fa = L_FULL + lane * 4;
    asm volatile("ds_write_b32 %0, %1\n\tds_write_b32 %0, %1 offset:128\n\ts_waitcnt lgkmcnt(0)\n\ts_barrier" ::"v"(fa), "v"(0u) : "memory");
    const int rr = lane >> 4, p = lane & 15;
    int vk[4], vv[2];
#pragma unroll
    for (int u = 0; u < 4; ++u) vk[u] = rr * 256 + ((p ^ (4 * u + rr)) << 4);
#pragma unroll
    for (int u = 0; u < 2; ++u) vv[u] = rr * 256 + ((p ^ (2 * (4 * u + rr))) << 4);
    int slot = 1;
    for (int z = 1; z <= NTILE; ++z) {
      const int so = (bb * NTILE + z - 1) * 8192;
      LAS char* dst = L + slot * SLOT;
      const int nslot = (slot + 1 == NS) ? 0 : slot + 1;
      unsigned fr;
#pragma unroll
      for (int u = 0; u < 16; ++u) {
        wait_vm<WIN - 1>();
        if (((u - WIN + 1) & 7) == 0) {
          const int v = (16 * z + u - WIN + 1) / 8;
          asm volatile("ds_write_b32 %0, %1" ::"v"(fa), "v"(v < 0 ? 0 : v) : "memory");
        }
        if (u < 8) __builtin_amdgcn_raw_ptr_buffer_load_lds(rk, dst + u * 1024, 16, vk[u & 3], so + u * 1024, 0, 2);
        else __builtin_amdgcn_raw_ptr_buffer_load_lds(rv, dst + u * 1024, 16, vv[u & 1], so + (u - 8) * 1024, 0, 2);
        if (u == 13) asm volatile("ds_read_b32 %0, %1" : "=v"(fr) : "v"(L_FREE + nslot * 4) : "memory");
      }
      asm volatile("s_waitcnt lgkmcnt(0)" : "+v"(fr)::"memory");
      if (z + 1 >= NS) {
        const unsigned need = z + 2 - NS;
        _Pragma("unroll 1") for (unsigned n = 0; n < SPIN_CAP && (unsigned)__builtin_amdgcn_readfirstlane(fr) < need; ++n)
          asm volatile("s_sleep 1\n\tds_read_b32 %0, %1\n\ts_waitcnt lgkmcnt(0)" : "=v"(fr) : "v"(L_FREE + nslot * 4) : "memory");
      }
      slot = nslot;
    }
    {
      auto r1 = __builtin_amdgcn_make_buffer_rsrc((void*)w1, 0, D * DFF * 2, 0x00020000);
      auto r2 = __builtin_amdgcn_make_buffer_rsrc((void*)w2, 0, D * DFF * 2, 0x00020000);
      auto ro = __builtin_amdgcn_make_buffer_rsrc((void*)wo, 0, D * HQ * DH * 2, 0x00020000);
      const int f0 = (xcd * 28 + (r32 < 28 ? r32 : 27)) * 8;
      LAS char* d1 = L + ((NTILE + 1) % NS) * SLOT;
      LAS char* d2 = L + ((NTILE + 2) % NS) * SLOT;
      constexpr int W2 = (WIN + 32 <= 63) ? 63 : WIN;
      constexpr int I0 = 16 * (NTILE + 1);
      constexpr int PUB0 = (I0 - 1 - WIN + 1) / 8;
#pragma unroll
      for (int u = 0; u < 32; ++u) {
        if (W2 != 63) {
          wait_vm<W2 - 1>();
          if (((I0 + u - W2 + 1) & 7) == 0 && (I0 + u - W2 + 1) / 8 <= 2 * NTILE + 2) asm volatile("ds_write_b32 %0, %1" ::"v"(fa), "v"((I0 + u - W2 + 1) / 8) : "memory");
        }
        if (u == 16) {
          unsigned fr = 0;
          _Pragma("unroll 1") for (unsigned n = 0; n < SPIN_CAP && (unsigned)__builtin_amdgcn_readfirstlane(fr) < NTILE + 3 - NS; ++n)
            asm volatile("ds_read_b32 %0, %1\n\ts_waitcnt lgkmcnt(0)" : "=v"(fr) : "v"(L_FREE + ((NTILE + 2) % NS) * 4) : "memory");
        }
        if (u < 8) __builtin_amdgcn_raw_ptr_buffer_load_lds(r1, d1 + u * 1024, 16, lane * (DFF * 2), f0 * 2 + u * 64 * (DFF * 2), 0, AUXW);
        else if (u < 16) __builtin_amdgcn_raw_ptr_buffer_load_lds(r2, d1 + u * 1024, 16, lane * 16, (f0 + u - 8) * 1024, 0, 2);
        else __builtin_amdgcn_raw_ptr_buffer_load_lds(ro, d2 + (u - 16) * 1024, 16, lane * 16, (bb * 16 + u - 16) * 1024, 0, 2);
      }
      if (2 * NTILE - 1 > PUB0) { wait_vm<32 + 24>(); asm volatile("ds_write_b32 %0, %1" ::"v"(fa), "v"(2 * NTILE - 1) : "memory"); }
      if (2 * NTILE > PUB0) { wait_vm<32 + 16>(); asm volatile("ds_write_b32 %0, %1" ::"v"(fa), "v"(2 * NTILE) : "memory"); }
      wait_vm<32 + 8>(); asm volatile("ds_write_b32 %0, %1" ::"v"(fa), "v"(2 * NTILE + 1) : "memory");
      wait_vm<32>(); asm volatile("ds_write_b32 %0, %1" ::"v"(fa), "v"(2 * NTILE + 2) : "memory");
    }
    STAMP(1);
    bar();
    wait_vm<0>();
    bar();
    return;
  }

  const int cw = wave - 1, t = threadIdx.x - 64, grp = lane >> 4, i = lane & 15;
  if (cw == 0) STAMP(0);
  auto rw = __builtin_amdgcn_make_buffer_rsrc((void*)ws, 0, W_END, 0x00020000);
  const v2u E = __builtin_amdgcn_raw_buffer_load_b64(rw, W_E, 0, 16);
  v4u xv;
  if (cw == 0) xv = *(const v4u*)(x + lane * 8);
  bar();
  const unsigned tagA = E[0] + 1u, tagB = E[1] + 1u;

  if (cw == 0) {
    float xf[8];
#pragma unroll
    for (int k = 0; k < 4; ++k) { xf[2 * k] = lo16(xv[k]); xf[2 * k + 1] = hi16(xv[k]); }
    float s = 0.f;
#pragma unroll
    for (int k = 0; k < 8; ++k) s += xf[k];
    const float mean = wave_sum(s) * (1.f / D);
    float v = 0.f;
#pragma unroll
    for (int k = 0; k < 8; ++k) { const float d = xf[k] - mean; v += d * d; }
    const float var = wave_sum(v) * (1.f / D);
    const float mb = rbf(mean), den = rbf(sqrtf(rbf(rbf(var) + 1e-5f)));
    v4u xn;
#pragma unroll
    for (int k = 0; k < 4; ++k) xn[k] = cvtpk(rbf(xf[2 * k] - mb) / den, rbf(xf[2 * k + 1] - mb) / den);
    *(LAS v4u*)(L + L_XN + lane * 16) = xn;
    lds_wait_ge(L, L_FULL, 2);
    STAMP(2);
    f32x4 a0 = {0.f, 0.f, 0.f, 0.f}, a1 = {0.f, 0.f, 0.f, 0.f};
    LAS char* wp = L + (8 * grp + (i >> 2)) * 32 + (i & 3) * 8;
#pragma unroll
    for (int s2 = 0; s2 < 16; ++s2) {
      const v2u e0 = trread(wp + s2 * 1024), e1 = trread(wp + s2 * 1024 + 128);
      const v4u xb = *(LAS v4u*)(L + L_XN + s2 * 64 + grp * 16);
      if (s2 & 1) a1 = mfma(v4u{e0[0], e0[1], e1[0], e1[1]}, xb, a1);
      else a0 = mfma(v4u{e0[0], e0[1], e1[0], e1[1]}, xb, a0);
    }
    a0 += a1;
    if (i == 0) __builtin_amdgcn_raw_buffer_store_b128(v4u{cvtpk(a0[0], a0[1]), tagA, cvtpk(a0[2], a0[3]), tagB}, rw, W_Q + bb * 64 + grp * 16, 0, 16);
    asm volatile("" ::: "memory");
    if (lane == 0) *(volatile LAS unsigned*)(L + L_FREE) = 1u;
    STAMP(3);
    v4u q0, q1;
    _Pragma("unroll 1") for (unsigned n = 0; n < SPIN_CAP; ++n) {
      q0 = __builtin_amdgcn_raw_buffer_load_b128(rw, W_Q + g * 2048 + lane * 32, 0, 16);
      q1 = __builtin_amdgcn_raw_buffer_load_b128(rw, W_Q + g * 2048 + lane * 32 + 16, 0, 16);
      const bool ok = q0[1] == tagA && q0[3] == tagB && q1[1] == tagA && q1[3] == tagB;
      if (__all(ok)) break;
      asm volatile("s_sleep 1" ::: "memory");
    }
    *(LAS v4u*)(L + L_QB + lane * 16) = v4u{q0[0], q0[2], q1[0], q1[2]};
    asm volatile("s_waitcnt lgkmcnt(0)" ::: "memory");
    if (lane == 0) *(volatile LAS unsigned*)(L + L_QRDY) = 1u;
    STAMP(4);
  }

  v4u qf[4];
  f32x4 acc[8];
#pragma unroll
  for (int n = 0; n < 8; ++n) acc[n] = f32x4{0.f, 0.f, 0.f, 0.f};
  float m = -INFINITY, l = 0.f;
  const int ka0 = i * 256 + ((grp ^ i) << 4);
  const int va0 = 8192 + (4 * grp + (i >> 2)) * 256 + ((((i & 3) >> 1) ^ (2 * (4 * (grp & 1) + (i >> 2)))) << 4) + (i & 1) * 8;
  constexpr float LOG2E = 1.4426950408889634f, SCALE = 0.08838834764831845f;
  int slot = (cw == 0 ? 4 : cw);
  bool first = true;
  for (int z = slot; z <= NTILE; z += 4) {
    LAS char* S = L + slot * SLOT;
    v4u kf[2][4];
    v2u vf[8][2];
    lds_wait_ge(L, L_FULL, 2 * z + 1);
#pragma unroll
    for (int s = 0; s < 4; ++s)
#pragma unroll
      for (int u = 0; u < 2; ++u) kf[u][s] = *(LAS v4u*)(S + (ka0 ^ (s * 64)) + u * 4096);
    if (first) {
      lds_wait_ge(L, L_FULL, 2 * z + 2);
#pragma unroll
      for (int n = 0; n < 8; ++n)
#pragma unroll
        for (int u = 0; u < 2; ++u) vf[n][u] = trread(S + (va0 ^ (n * 32)) + u * 4096);
      asm volatile("s_waitcnt lgkmcnt(0)" ::: "memory");
      if (lane == 0) *(volatile LAS unsigned*)(L + L_FREE + slot * 4) = z + 1;
      lds_wait_ge(L, L_QRDY, 1);
#pragma unroll
      for (int s = 0; s < 4; ++s) qf[s] = *(LAS v4u*)(L + L_QB + (i & 3) * 256 + s * 64 + grp * 16);
    }
    f32x4 sc[2];
#pragma unroll
    for (int u = 0; u < 2; ++u) {
      sc[u] = f32x4{0.f, 0.f, 0.f, 0.f};
#pragma unroll
      for (int s = 0; s < 4; ++s) sc[u] = mfma(kf[u][s], qf[s], sc[u]);
    }
    float xs[8];
#pragma unroll
    for (int k = 0; k < 4; ++k) {
      const unsigned r0 = cvtpk(sc[k >> 1][(k & 1) * 2], sc[k >> 1][(k & 1) * 2 + 1]);
      const unsigned r1 = cvtpk(lo16(r0) * SCALE, hi16(r0) * SCALE);
      xs[2 * k] = lo16(r1); xs[2 * k + 1] = hi16(r1);
    }
    float tm = fmaxf(fmaxf(fmaxf(xs[0], xs[1]), fmaxf(xs[2], xs[3])), fmaxf(fmaxf(xs[4], xs[5]), fmaxf(xs[6], xs[7])));
    tm = x16_32_max(tm);
    const float mn = fmaxf(m, tm), alpha = __builtin_amdgcn_exp2f((m - mn) * LOG2E);
    m = mn;
    float pe[8], ps = 0.f;
#pragma unroll
    for (int k = 0; k < 8; ++k) { pe[k] = __builtin_amdgcn_exp2f((xs[k] - mn) * LOG2E); ps += pe[k]; }
    l = l * alpha + ps;
    const v4u pf = {cvtpk(pe[0], pe[1]), cvtpk(pe[2], pe[3]), cvtpk(pe[4], pe[5]), cvtpk(pe[6], pe[7])};
#pragma unroll
    for (int n = 0; n < 8; ++n) acc[n] *= alpha;
    if (!first) {
      lds_wait_ge(L, L_FULL, 2 * z + 2);
#pragma unroll
      for (int n = 0; n < 8; ++n)
#pragma unroll
        for (int u = 0; u < 2; ++u) vf[n][u] = trread(S + (va0 ^ (n * 32)) + u * 4096);
      asm volatile("s_waitcnt lgkmcnt(0)" ::: "memory");
      if (lane == 0) *(volatile LAS unsigned*)(L + L_FREE + slot * 4) = z + 1;
    }
#pragma unroll
    for (int n = 0; n < 8; ++n) acc[n] = mfma(v4u{vf[n][0][0], vf[n][0][1], vf[n][1][0], vf[n][1][1]}, pf, acc[n]);
    first = false;
    slot += 4; if (slot >= NS) slot -= NS;
  }
  l = x16_32_sum(l);
  if (i < 4) {
#pragma unroll
    for (int n = 0; n < 8; ++n) *(LAS f32x4*)(L + L_MRG + (cw * 4 + i) * 512 + (16 * n + 4 * grp) * 4) = acc[n];
    if (grp == 0) *(LAS f32x2*)(L + L_ML + (i * 4 + cw) * 8) = f32x2{m, l};
  }
  if (cw == 0) STAMP(5);
  bar();
  {
    const f32x4 ml0 = *(LAS f32x4*)(L + L_ML + cw * 32), ml1 = *(LAS f32x4*)(L + L_ML + cw * 32 + 16);
    const float M = fmaxf(fmaxf(ml0[0], ml0[2]), fmaxf(ml1[0], ml1[2]));
    const float e0 = __builtin_amdgcn_exp2f((ml0[0] - M) * LOG2E), e1 = __builtin_amdgcn_exp2f((ml0[2] - M) * LOG2E),
                e2 = __builtin_amdgcn_exp2f((ml1[0] - M) * LOG2E), e3 = __builtin_amdgcn_exp2f((ml1[2] - M) * LOG2E);
    const f32x2 o0 = *(LAS f32x2*)(L + L_MRG + (0 + cw) * 512 + lane * 8), o1 = *(LAS f32x2*)(L + L_MRG + (4 + cw) * 512 + lane * 8),
                o2 = *(LAS f32x2*)(L + L_MRG + (8 + cw) * 512 + lane * 8), o3 = *(LAS f32x2*)(L + L_MRG + (12 + cw) * 512 + lane * 8);
    const f32x2 o = o0 * e0 + o1 * e1 + o2 * e2 + o3 * e3;
    const float Ls = ml0[1] * e0 + ml0[3] * e1 + ml1[1] * e2 + ml1[3] * e3;
    __builtin_amdgcn_raw_buffer_store_b128(v4u{__float_as_uint(o[0]), tagA, __float_as_uint(o[1]), tagB}, rw, W_O + bb * 4096 + t * 16, 0, 16);
    if (lane == 0) __builtin_amdgcn_raw_buffer_store_b128(v4u{__float_as_uint(M), tagA, __float_as_uint(Ls), tagB}, rw, W_ML + bb * 128 + cw * 16, 0, 16);
  }
  if (cw == 0) STAMP(6);
  v4u go[4], gm[4];
  const int p8 = lane >> 3, k8 = lane & 7;
  const int ao = W_O + (g * 32 + p8) * 4096 + (8 * j + k8) * 16, am = W_ML + (g * 32 + p8) * 128 + (j >> 3) * 16;
  if (cw == 0) {
#pragma unroll
    for (int it = 0; it < 4; ++it) { go[it] = __builtin_amdgcn_raw_buffer_load_b128(rw, ao + it * 8 * 4096, 0, 16); gm[it] = __builtin_amdgcn_raw_buffer_load_b128(rw, am + it * 8 * 128, 0, 16); }
  }
  bar();
  unsigned wr[24];
  {
    LAS char* d1 = L + ((NTILE + 1) % NS) * SLOT;
    LAS char* d2 = L + ((NTILE + 2) % NS) * SLOT;
#pragma unroll
    for (int u = 0; u < 16; ++u) wr[u] = *(LAS unsigned*)(d2 + u * 1024 + t * 4);
#pragma unroll
    for (int u = 0; u < 8; ++u) wr[16 + u] = *(LAS unsigned*)(d1 + 8192 + u * 1024 + t * 4);
    if (cw == 1) {
      f32x4 a0 = {0.f, 0.f, 0.f, 0.f}, a1 = {0.f, 0.f, 0.f, 0.f};
      LAS char* wp = d1 + (8 * grp + (i >> 2)) * 16 + (i & 1) * 8;
#pragma unroll
      for (int s2 = 0; s2 < 16; ++s2) {
        const v2u e0 = trread(wp + s2 * 512), e1 = trread(wp + s2 * 512 + 64);
        const v4u xb = *(LAS v4u*)(L + L_XN + s2 * 64 + grp * 16);
        if (s2 & 1) a1 = mfma(v4u{e0[0], e0[1], e1[0], e1[1]}, xb, a1);
        else a0 = mfma(v4u{e0[0], e0[1], e1[0], e1[1]}, xb, a0);
      }
      a0 += a1;
      f32x4 h;
#pragma unroll
      for (int k = 0; k < 4; ++k) h[k] = r32 < 28 ? fmaxf(rbf(a0[k]), 0.f) : 0.f;
      if (i == 0 && grp < 2) *(LAS f32x4*)(L + L_OV + 64 + grp * 16) = h;
    }
  }
  if (cw == 0) {
    _Pragma("unroll 1") for (unsigned n = 0; n < SPIN_CAP; ++n) {
      bool ok = true;
#pragma unroll
      for (int it = 0; it < 4; ++it) ok = ok && go[it][1] == tagA && go[it][3] == tagB && gm[it][1] == tagA && gm[it][3] == tagB;
      if (__all(ok)) break;
      asm volatile("" ::: "memory");
#pragma unroll
      for (int it = 0; it < 4; ++it) { go[it] = __builtin_amdgcn_raw_buffer_load_b128(rw, ao + it * 8 * 4096, 0, 16); gm[it] = __builtin_amdgcn_raw_buffer_load_b128(rw, am + it * 8 * 128, 0, 16); }
    }
    STAMP(7);
    float M = fmaxf(fmaxf(__uint_as_float(gm[0][0]), __uint_as_float(gm[1][0])), fmaxf(__uint_as_float(gm[2][0]), __uint_as_float(gm[3][0])));
    M = fmaxf(M, dpp<ROR8>(M)); M = x16_32_max(M);
    float Ls = 0.f, o0 = 0.f, o1 = 0.f;
#pragma unroll
    for (int it = 0; it < 4; ++it) {
      const float e = __builtin_amdgcn_exp2f((__uint_as_float(gm[it][0]) - M) * LOG2E);
      Ls += e * __uint_as_float(gm[it][2]); o0 += e * __uint_as_float(go[it][0]); o1 += e * __uint_as_float(go[it][2]);
    }
    Ls += dpp<ROR8>(Ls); o0 += dpp<ROR8>(o0); o1 += dpp<ROR8>(o1);
    Ls = x16_32_sum(Ls); o0 = x16_32_sum(o0); o1 = x16_32_sum(o1);
    if (lane < 8) *(LAS f32x2*)(L + L_OV + lane * 8) = f32x2{rbf(o0 / Ls), rbf(o1 / Ls)};
  }
  bar();
  float y0 = 0.f, y1 = 0.f;
#pragma unroll
  for (int u = 0; u < 6; ++u) {
    const f32x4 c = *(LAS f32x4*)(L + L_OV + u * 16);
#pragma unroll
    for (int k = 0; k < 4; ++k) { y0 = fmaf(c[k], lo16(wr[4 * u + k]), y0); y1 = fmaf(c[k], hi16(wr[4 * u + k]), y1); }
  }
  __builtin_amdgcn_raw_buffer_store_b128(v4u{__float_as_uint(y0), tagA, __float_as_uint(y1), tagB}, rw, W_P + b * 4096 + t * 16, 0, 16);
  if (cw == 0) STAMP(8);
  const int pc = xcd * 32 + r32;
  const unsigned xr = *(const unsigned*)(x + 2 * pc);
  v4u gp;
  _Pragma("unroll 1") for (unsigned n = 0; n < SPIN_CAP; ++n) {
    gp = __builtin_amdgcn_raw_buffer_load_b128(rw, W_P + t * 4096 + pc * 16, 0, 16);
    if (__all(gp[1] == tagA && gp[3] == tagB)) break;
    asm volatile("" ::: "memory");
  }
  y0 = wave_sum(__uint_as_float(gp[0])); y1 = wave_sum(__uint_as_float(gp[2]));
  if (lane == 0) *(LAS f32x2*)(L + L_RED + cw * 8) = f32x2{y0, y1};
  bar();
  if (t == 0) {
    const f32x4 s0 = *(LAS f32x4*)(L + L_RED), s1 = *(LAS f32x4*)(L + L_RED + 16);
    *(unsigned*)(out + 2 * pc) = cvtpk(lo16(xr) + ((s0[0] + s0[2]) + (s1[0] + s1[2])), hi16(xr) + ((s0[1] + s0[3]) + (s1[1] + s1[3])));
    if (b == 0) __builtin_amdgcn_raw_buffer_store_b64(v2u{tagA, tagB}, rw, W_E, 0, 16);
  }
  if (cw == 0) STAMP(9);
}

extern "C" void kernel_launch(void* const* d_in, const int* in_sizes, int n_in, void* d_out, int out_size, void* d_ws, size_t ws_size, hipStream_t stream) {
  (void)hipFuncSetAttribute((const void*)fused_decode, hipFuncAttributeMaxDynamicSharedMemorySize, L_END);
  fused_decode<<<NWG, NTHR, L_END, stream>>>((const u16*)d_in[0], (const u16*)d_in[1], (const u16*)d_in[2], (const u16*)d_in[3], (const u16*)d_in[4],
                                             (const u16*)d_in[6], (const u16*)d_in[7], (u16*)d_out, (char*)d_ws);
}
```

```cpp
#include <hip/hip_runtime.h>
#include <stdint.h>

#define DBG 0
#define WA_ 27
#define WB_ 23
#define WC_ 21
#define WD_ 19
#define WE_ 17
#define LEV0 2
#define BAND 30
#define ADAPT 1
#define TAU16 875
#define CK0 8
#define CKS 16
#define CKD 4
#define ROTM 0
#define OWND 25
#define XQ 0
#define PAT 1
#define ASG 0
#define CODE_KB 0
#define PACE 1
#define QDELAY 120
#define C1 110
#define D2 40
#define MLLEAD 45
#define MAPB 0

#define LAS __attribute__((address_space(3)))
typedef unsigned v4u __attribute__((ext_vector_type(4)));
typedef unsigned v2u __attribute__((ext_vector_type(2)));
typedef float f32x4 __attribute__((ext_vector_type(4)));
typedef float f32x2 __attribute__((ext_vector_type(2)));
typedef __bf16 bf16x8 __attribute__((ext_vector_type(8)));
typedef __bf16 bf16x2 __attribute__((ext_vector_type(2)));
typedef short s16x4 __attribute__((ext_vector_type(4)));
typedef unsigned short u16;

constexpr int D = 512, HQ = 32, HKV = 8, SK = 65536, DH = 128, DFF = 1792;
constexpr int NWG = 256, NTHR = 384, NTILE = 64;
constexpr int NS = 9, SLOT = 16384;
constexpr int L_XN = NS * SLOT, L_QB = L_XN + 1024, L_STG = L_QB + 1024, L_PX = L_STG + 8192, L_ML = L_PX + 4096, L_OV = L_ML + 1024,
              L_FULL = L_OV + 128, L_FREE = L_FULL + 256, L_QRDY = L_FREE + 64, L_HRDY = L_QRDY + 4, L_SY1 = L_QRDY + 8, L_SY2 = L_QRDY + 12,
              L_LEV = L_FREE + NS * 4, L_END = L_QRDY + 64;
static_assert(L_END <= 163840, "LDS");
constexpr int W_E = 0, W_ML = 4096, W_T = 40960, W_Q = 65536, W_H = W_Q + 131072, W_O = W_H + 131072, W_P = W_O + (1 << 20), W_END = W_P + (1 << 20), W_DBG = 4 << 20;
constexpr unsigned SPIN_CAP = 1u << 16;
constexpr float LOG2E = 1.4426950408889634f, SCALE = 0.08838834764831845f;

__device__ __forceinline__ unsigned cvtpk(float lo, float hi) { return __builtin_bit_cast(unsigned, __builtin_convertvector(f32x2{lo, hi}, bf16x2)); }
__device__ __forceinline__ float lo16(unsigned u) { return __uint_as_float(u << 16); }
__device__ __forceinline__ float hi16(unsigned u) { return __uint_as_float(u & 0xffff0000u); }
__device__ __forceinline__ float rbf(float x) { return lo16(cvtpk(x, x)); }
template <int N> __device__ __forceinline__ void wait_vm() { asm volatile("s_waitcnt vmcnt(%0)" ::"n"(N) : "memory"); }
template <int CTRL> __device__ __forceinline__ float dpp(float x) { return __builtin_bit_cast(float, __builtin_amdgcn_mov_dpp(__builtin_bit_cast(int, x), CTRL, 0xf, 0xf, true)); }
constexpr int XOR1 = 0xB1, XOR2 = 0x4E, XOR7 = 0x141, ROR8 = 0x128, ROR4 = 0x124;
__device__ __forceinline__ float x16_32_max(float x) {
  auto s = __builtin_amdgcn_permlane16_swap(__float_as_uint(x), __float_as_uint(x), false, false);
  x = fmaxf(__uint_as_float(s[0]), __uint_as_float(s[1]));
  auto t = __builtin_amdgcn_permlane32_swap(__float_as_uint(x), __float_as_uint(x), false, false);
  return fmaxf(__uint_as_float(t[0]), __uint_as_float(t[1]));
}
__device__ __forceinline__ float x16_32_sum(float x) {
  auto s = __builtin_amdgcn_permlane16_swap(__float_as_uint(x), __float_as_uint(x), false, false);
  x = __uint_as_float(s[0]) + __uint_as_float(s[1]);
  auto t = __builtin_amdgcn_permlane32_swap(__float_as_uint(x), __float_as_uint(x), false, false);
  return __uint_as_float(t[0]) + __uint_as_float(t[1]);
}
__device__ __forceinline__ float wave_sum(float x) {
  x += dpp<XOR1>(x); x += dpp<XOR2>(x); x += dpp<XOR7>(x); x += dpp<ROR8>(x);
  return x16_32_sum(x);
}
__device__ __forceinline__ f32x4 mfma(v4u a, v4u b, f32x4 c) { return __builtin_amdgcn_mfma_f32_16x16x32_bf16(__builtin_bit_cast(bf16x8, a), __builtin_bit_cast(bf16x8, b), c, 0, 0, 0); }
__device__ __forceinline__ v2u trread(LAS char* p) { return __builtin_bit_cast(v2u, __builtin_amdgcn_ds_read_tr16_b64_v4i16((LAS s16x4*)p)); }
__device__ __forceinline__ void bar() { asm volatile("s_waitcnt lgkmcnt(0)\n\ts_barrier" ::: "memory"); }
template <int SL> __device__ __forceinline__ unsigned lds_wait(int off, unsigned need) {
  unsigned v, n;
  if (SL == 0)
    asm volatile("s_mov_b32 %1, %4\n.Lw%=:\n\tds_read_b32 %0, %2\n\ts_sub_u32 %1, %1, 1\n\ts_waitcnt lgkmcnt(0)\n\tv_cmp_le_u32 vcc, %3, %0\n\ts_cbranch_scc1 .Lx%=\n\ts_cbranch_vccz .Lw%=\n.Lx%=:"
                 : "=&v"(v), "=&s"(n) : "v"(off), "s"(need), "n"(SPIN_CAP) : "memory", "vcc", "scc");
  else
    asm volatile("s_mov_b32 %1, %4\n.Lw%=:\n\tds_read_b32 %0, %2\n\ts_waitcnt lgkmcnt(0)\n\tv_cmp_le_u32 vcc, %3, %0\n\ts_cbranch_vccnz .Lx%=\n\ts_sleep %5\n\ts_sub_u32 %1, %1, 1\n\ts_cbranch_scc0 .Lw%=\n.Lx%=:"
                 : "=&v"(v), "=&s"(n) : "v"(off), "s"(need), "n"(SPIN_CAP), "n"(SL) : "memory", "vcc", "scc");
  return v;
}
#define STAMP(k) do {} while (0)
#define FLUSH() do {} while (0)
#define TAGS_BAD(v) (((v)[1] ^ tagA) | ((v)[3] ^ tagB))
#define PIN4(a) asm volatile("" : "+v"(a))

__device__ __forceinline__ void qk_scores(const v4u (&kf)[2][4], const v4u (&qf)[4], float* xs) {
  f32x4 s0 = {0.f, 0.f, 0.f, 0.f}, s1 = {0.f, 0.f, 0.f, 0.f};
#pragma unroll
  for (int s = 0; s < 4; ++s) { s0 = mfma(kf[0][s], qf[s], s0); s1 = mfma(kf[1][s], qf[s], s1); }
#pragma unroll
  for (int k = 0; k < 4; ++k) {
    const f32x4 sc = k < 2 ? s0 : s1;
    const unsigned r0 = cvtpk(sc[(k & 1) * 2], sc[(k & 1) * 2 + 1]);
    const unsigned r1 = cvtpk(lo16(r0) * SCALE, hi16(r0) * SCALE);
    xs[2 * k] = lo16(r1); xs[2 * k + 1] = hi16(r1);
  }
}
__device__ __forceinline__ void read_k(LAS char* S, int ka0, v4u (&kf)[2][4]) {
#pragma unroll
  for (int s = 0; s < 4; ++s)
#pragma unroll
    for (int u = 0; u < 2; ++u) kf[u][s] = *(LAS v4u*)(S + (ka0 ^ (s * 64)) + u * 4096);
}
__device__ __forceinline__ void read_v(LAS char* S, int va0, v4u (&vf)[8]) {
#pragma unroll
  for (int n = 0; n < 8; ++n) {
    const v2u a = trread(S + (va0 ^ (n * 32))), c = trread(S + (va0 ^ (n * 32)) + 4096);
    vf[n] = v4u{a[0], a[1], c[0], c[1]};
  }
}
__device__ __forceinline__ void mask_p(const float* pe, int c4, v4u (&pm)[4]) {
  const v4u pf = {cvtpk(pe[0], pe[1]), cvtpk(pe[2], pe[3]), cvtpk(pe[4], pe[5]), cvtpk(pe[6], pe[7])};
#pragma unroll
  for (int c = 0; c < 4; ++c) pm[c] = c4 == c ? pf : v4u{0u, 0u, 0u, 0u};
}
__device__ __forceinline__ void pv(const v4u (&vf)[8], const v4u (&pm)[4], f32x4& a0, f32x4& a1) {
#pragma unroll
  for (int c = 0; c < 4; ++c) { a0 = mfma(vf[2 * c], pm[c], a0); a1 = mfma(vf[2 * c + 1], pm[c], a1); }
}
template <int NST>
__device__ __forceinline__ f32x2 slice_matvec(LAS char* W, LAS char* xn, int grp, int i) {
  const int base = (8 * grp + (i >> 2)) * 128 + ((((i & 3) >> 1) ^ (2 * (((i >> 3) & 1) | ((grp & 1) << 1)))) << 4) + (i & 1) * 8;
  f32x4 a[4];
#pragma unroll
  for (int ct = 0; ct < 4; ++ct) a[ct] = f32x4{0.f, 0.f, 0.f, 0.f};
#pragma unroll
  for (int s = 0; s < NST; ++s) {
    const v4u xb = *(LAS v4u*)(xn + s * 64 + grp * 16);
#pragma unroll
    for (int ct = 0; ct < 4; ++ct) {
      const v2u e0 = trread(W + (base ^ (ct * 32)) + s * 4096), e1 = trread(W + (base ^ (ct * 32)) + s * 4096 + 512);
      a[ct] = mfma(v4u{e0[0], e0[1], e1[0], e1[1]}, xb, a[ct]);
    }
  }
  const f32x4 lo = (i & 2) ? a[1] : a[0], hi = (i & 2) ? a[3] : a[2], v = (i & 4) ? hi : lo;
  return (i & 1) ? f32x2{v[2], v[3]} : f32x2{v[0], v[1]};
}

__device__ __forceinline__ unsigned now32() { return (unsigned)__builtin_amdgcn_s_memrealtime(); }
__device__ __forceinline__ void wait_until(unsigned t) {
  unsigned long long c; unsigned n;
  t = __builtin_amdgcn_readfirstlane(t);
  asm volatile("s_mov_b32 %1, %3\n.Lw%=:\n\ts_memrealtime vcc\n\ts_sub_u32 %1, %1, 1\n\ts_cbranch_scc1 .Lx%=\n\ts_waitcnt lgkmcnt(0)\n\ts_sub_u32 vcc_lo, vcc_lo, %2\n\ts_cmp_lt_i32 vcc_lo, 0\n\ts_cbranch_scc1 .Lw%=\n.Lx%=:\n\ts_waitcnt lgkmcnt(0)"
               : "=&s"(c), "=&s"(n) : "s"(t), "n"(SPIN_CAP) : "memory", "vcc", "scc");
}
__device__ __forceinline__ void sync4(LAS char* L, int off, int lane) {
  asm volatile("" ::: "memory");
  if (lane == 0) __hip_atomic_fetch_add((LAS unsigned*)(L + off), 1u, __ATOMIC_RELAXED, __HIP_MEMORY_SCOPE_WORKGROUP);
  lds_wait<0>(off, 4u);
}
__device__ __forceinline__ int swz(int row) { return 2 * ((row & 7) ^ (4 * (row >> 3))) + (row >> 3); }

constexpr int STEP_SH = PAT == 0 ? 0 : PAT == 1 ? 4 : 1, STEP = 4096 << STEP_SH;
__device__ __forceinline__ int tile_off(int j, int n) {
  const int k = n & 15, ph = n >> 4;
  const int in = PAT == 0 ? k << 13 : PAT == 1 ? ((k & 1) << 13) | ((k & 2) << 11) | ((k & 12) << 12) : ((k & 1) << 16) | ((k & 2) << 11) | ((k & 12) << 12);
  return in + (ASG == 0 ? (j << 19) + (((ph + (j & ROTM)) & 3) << 17) : (((j >> 4) * 4 + ph) << 21) + ((j & 15) << 17));
}

#define STR_(x) #x
#define STR(x) STR_(x)
#define GATE(W) "s_waitcnt vmcnt(" STR(W) "-2)\n\tds_write_b32 %[fa], %[ld]\n\tv_add_u32 %[ld], 2, %[ld]\n\t"
#define LD2A(so) "buffer_load_dwordx4 %[v0], " so " offen nt lds\n\tbuffer_load_dwordx4 %[v1], " so " offen offset:1024 nt lds\n\t"
#define LD2B(so) "buffer_load_dwordx4 %[v2], " so " offen offset:2048 nt lds\n\tbuffer_load_dwordx4 %[v3], " so " offen offset:3072 nt lds\n\t"
#define FILL(W) \
  "s_lshl_b32 s53, s52, 4\n\ts_mov_b32 m0, s48\n\ts_add_u32 s53, s53, 66-" STR(W) "\n\tv_mov_b32 %[ld], s53\n\t" \
  GATE(W) LD2A("s[40:43], 0") \
  "ds_read_b32 %[fr], %[fra]\n\ts_add_u32 s52, s52, 1\n\ts_and_b32 s54, s52, 63\n\t" \
  GATE(W) LD2B("s[40:43], 0") \
  "s_add_u32 m0, s48, s63\n\t" \
  GATE(W) LD2A("s[40:43], s50") \
  "v_readlane_b32 s61, %[tp], s54\n\t" \
  GATE(W) LD2B("s[40:43], s50") \
  "s_mov_b32 m0, s49\n\t" \
  GATE(W) LD2A("s[44:47], 0") \
  "v_readlane_b32 s40, %[ta0], s54\n\tv_readlane_b32 s41, %[ta1], s54\n\t" \
  "s_and_b32 s48, s61, 0xff\n\ts_lshl_b32 s48, s48, 10\n\ts_bfe_u32 s50, s61, 0x30018\n\ts_lshl_b32 s50, s63, s50\n\ts_add_u32 s42, s50, s63\n\t" \
  GATE(W) LD2B("s[44:47], 0") \
  "s_add_u32 m0, s49, s63\n\ts_bfe_u32 s60, s61, 0x80010\n\ts_sub_u32 s60, s60, 16\n\ts_waitcnt lgkmcnt(2)\n\tv_readlane_b32 s53, %[fr], 9\n\tv_cmp_gt_i32 vcc, s60, %[fr]\n\t" \
  GATE(W) LD2A("s[44:47], s51") \
  "s_sub_u32 s53, s53, 0x400\n\ts_min_u32 s53, s53, 4\n\ts_mul_i32 s53, s53, s64\n\t" \
  "s_cmpk_eq_u32 s52, 67\n\ts_cselect_b32 s53, s66, s53\n\t" \
  "s_add_u32 s56, s58, s53\n\ts_addc_u32 s57, s59, 0\n\t" \
  GATE(W) LD2B("s[44:47], s51") \
  "s_bfe_u32 s49, s61, 0x80008\n\ts_lshl_b32 s49, s49, 10\n\tv_readlane_b32 s44, %[tb0], s54\n\tv_readlane_b32 s45, %[tb1], s54\n\t" \
  "s_mov_b32 s51, s50\n\ts_bitcmp1_b32 s61, 27\n\ts_cselect_b32 s46, 0, s42\n\t" \
  "s_cbranch_vccnz .Lstall%=\n\ts_setpc_b64 s[56:57]\n"

__global__ void __launch_bounds__(NTHR) fused_decode(const u16* __restrict__ x, const u16* __restrict__ Kc, const u16* __restrict__ Vc, const u16* __restrict__ wq,
                                                     const u16* __restrict__ wo, const u16* __restrict__ w1, const u16* __restrict__ w2, u16* __restrict__ out, char* ws) {
  extern __shared__ __attribute__((aligned(16))) char lds_raw[];
  LAS char* L = (LAS char*)lds_raw;
  const int b = blockIdx.x, lane = threadIdx.x & 63, wave = __builtin_amdgcn_readfirstlane(threadIdx.x >> 6);
  const int xcd = b & 7, r32 = b >> 3;
  if (CODE_KB) {
    unsigned long long pc; asm volatile("s_getpc_b64 %0" : "=s"(pc));
    auto rc = __builtin_amdgcn_make_buffer_rsrc((void*)(pc & ~255ull), 0, CODE_KB * 1024, 0x00020000);
    __builtin_amdgcn_raw_ptr_buffer_load_lds(rc, L + L_PX, 16, r32 * 1024 + threadIdx.x * 16 + (threadIdx.x >> 6) * 31 * 1024, 0, 0, 0);
  }
  asm volatile("ds_write_b32 %0, %1\n\tds_write_b32 %0, %2 offset:128" ::"v"(L_FULL + lane * 4), "v"(0u), "v"(lane < 32 || lane >= 32 + NS + 1 ? 0 : lane == 32 + NS ? 0x400 + LEV0 : lane - 32 - (NS - 1)));
  const int g = xcd, j = r32;
  const int bb = g * 32 + j;
  const int grp = lane >> 4, i = lane & 15;
  int vo[4];
#pragma unroll
  for (int u = 0; u < 4; ++u) vo[u] = grp * 256 + ((i ^ swz(4 * u + grp)) << 4);
  {
    auto rk = __builtin_amdgcn_make_buffer_rsrc((void*)Kc, 0, HKV * SK * DH * 2, 0x00020000);
    auto rv = __builtin_amdgcn_make_buffer_rsrc((void*)Vc, 0, HKV * SK * DH * 2, 0x00020000);
    const int s1 = (g << 24) + tile_off(j, 0), s2 = (g << 24) + tile_off(j, 1);
#define LD4(m, r, so) "s_mov_b32 m0, " m "\n\ts_nop 0\n\t" LD2A(r ", " so) LD2B(r ", " so)
    asm volatile("s_waitcnt lgkmcnt(0)\n\ts_barrier\n\ts_cmp_lg_u32 %[wv], 0\n\ts_cbranch_scc1 .Lskip%=\n\ts_setprio 3\n\t"
                 LD4("0x4000", "%[rk]", "%[s1]") LD4("0x5000", "%[rk]", "%[s1b]") LD4("0x6000", "%[rv]", "%[s1]") LD4("0x7000", "%[rv]", "%[s1b]")
                 LD4("0x8000", "%[rk]", "%[s2]") LD4("0x9000", "%[rk]", "%[s2b]") LD4("0xa000", "%[rv]", "%[s2]") LD4("0xb000", "%[rv]", "%[s2b]")
                 "\n.Lskip%=:"
                 :: [wv] "s"(wave), [rk] "s"(rk), [rv] "s"(rv), [s1] "s"(s1), [s1b] "s"(s1 + STEP), [s2] "s"(s2), [s2b] "s"(s2 + STEP),
                    [v0] "v"(vo[0]), [v1] "v"(vo[1]), [v2] "v"(vo[2]), [v3] "v"(vo[3]), "s"(x), "s"(wq), "s"(wo), "s"(w1), "s"(w2), "s"(out), "s"(ws) : "memory", "scc");
  }
  const int ff = xcd * 28 + (r32 < 28 ? r32 : 27);
  constexpr int SL_WO = (NTILE + 1) % NS, SL_W2 = (NTILE + 2) % NS;
  constexpr int NZ = NTILE - 4;

  if (wave == 0) {
    const unsigned long long kb = (unsigned long long)Kc + (g << 24), vb = (unsigned long long)Vc + (g << 24);
    const int z = lane < 3 ? 64 + lane : lane;
    const bool last4 = z > NZ;
    const int t0 = last4 ? NZ + 1 + 2 * ((z - NZ - 1) & 1) : z, t1_ = last4 ? t0 + 1 : z;
    const bool av = z > NZ + 2, bv = !last4 || av;
    unsigned long long ta = (av ? vb : kb) + tile_off(j, t0 - 1), tb = (bv ? vb : kb) + tile_off(j, t1_ - 1);
    unsigned tp = ((t0 % NS) * 16 + (av ? 8 : 0)) | (((t1_ % NS) * 16 + (bv ? 8 : 0)) << 8) | (t1_ << 16) | (STEP_SH << 24);
    if (lane == 1) {
      ta = (unsigned long long)wo + ((4 * g) * 128 + 32 * (j >> 3) + 16 * (j & 1) + 4 * ((j & 7) >> 1)) * 1024; tb = ta + 256 * 1024;
      tp = (SL_WO * 16) | ((SL_WO * 16 + 8) << 8) | ((NTILE + 1) << 16) | (5 << 24);
    }
    if (lane == 2) {
      ta = tb = (unsigned long long)w2 + ff * 8192;
      tp = (SL_W2 * 16) | ((SL_W2 * 16 + 8) << 8) | ((NTILE + 2) << 16) | (1 << 27);
    }
    int ld, fr;
    static_assert(WA_ <= 32 && WB_ <= 32 && WC_ <= 32 && NS == 9 && NTILE == 64, "loader constants");
    asm volatile(
        "s_nop 1\n\tv_readlane_b32 s40, %[ta0], 3\n\tv_readlane_b32 s41, %[ta1], 3\n\tv_readlane_b32 s44, %[tb0], 3\n\tv_readlane_b32 s45, %[tb1], 3\n\t"
        "s_mov_b32 s42, %[stp]+0x1000\n\ts_mov_b32 s43, 0x20000\n\ts_mov_b32 s46, s42\n\ts_mov_b32 s47, s43\n\t"
        "s_mov_b32 s48, 0xc000\n\ts_mov_b32 s49, 0xe000\n\ts_mov_b32 s50, %[stp]\n\ts_mov_b32 s51, s50\n\ts_mov_b32 s52, 3\n\t"
        "s_movk_i32 s63, 0x1000\n\ts_mov_b32 s64, .Lv1%=-.Lv0%=\n\ts_mov_b32 s66, .Lexit%=-.Lv0%=\n\t"
        "s_getpc_b64 s[58:59]\n"
        ".Lv0%=:\n\t" FILL(WA_) ".Lv1%=:\n\t" FILL(WB_) FILL(WC_) FILL(WD_) FILL(WE_)
        ".Lstall%=:\n\ts_mov_b32 s53, %[cap]\n"
        ".Lst%=:\n\tds_read_b32 %[fr], %[fra]\n\ts_sub_u32 s53, s53, 1\n\ts_waitcnt lgkmcnt(0)\n\tv_cmp_gt_i32 vcc, s60, %[fr]\n\ts_cbranch_scc1 .Lsx%=\n\ts_cbranch_vccnz .Lst%=\n"
        ".Lsx%=:\n\ts_setpc_b64 s[56:57]\n"
        ".Lexit%=:\n\tv_mov_b32 %[ld], %[fin]\n\ts_waitcnt vmcnt(0)\n\tds_write_b32 %[fa], %[ld]\n\ts_waitcnt lgkmcnt(0)"
        : [ld] "=&v"(ld), [fr] "=&v"(fr)
        : [ta0] "v"((unsigned)ta), [ta1] "v"((unsigned)(ta >> 32) & 0xffff), [tb0] "v"((unsigned)tb), [tb1] "v"((unsigned)(tb >> 32) & 0xffff), [tp] "v"(tp),
          [fa] "v"(L_FULL + lane * 4), [fra] "v"(L_FREE + (lane <= NS ? lane : 0) * 4), [v0] "v"(vo[0]), [v1] "v"(vo[1]), [v2] "v"(vo[2]), [v3] "v"(vo[3]),
          [cap] "n"(SPIN_CAP), [stp] "n"(STEP), [fin] "n"(64 + 16 * (NTILE + 3))
        : "memory", "vcc", "scc", "s40", "s41", "s42", "s43", "s44", "s45", "s46", "s47", "s48", "s49", "s50", "s51", "s52", "s53", "s54", "s55", "s56", "s57", "s58", "s59",
          "s60", "s61", "s62", "s63", "s64", "s65", "s66");
    STAMP(10); FLUSH();
    return;
  }
  constexpr unsigned FE = 64 + 16 * (NTILE + 1);
  auto rw = __builtin_amdgcn_make_buffer_rsrc((void*)ws, 0, W_END, 0x00020000);
  v2u E = __builtin_amdgcn_raw_buffer_load_b64(rw, W_E, 0, 16);

  if (wave == 5) {
    STAMP(0);
    v4u xv = *(const v4u*)(x + lane * 8);
    const int pc = xcd * 32 + r32;
    unsigned xr = *(const unsigned*)(x + 2 * pc);
    auto rq = __builtin_amdgcn_make_buffer_rsrc((void*)wq, 0, D * HQ * DH * 2, 0x00020000);
    auto r1 = __builtin_amdgcn_make_buffer_rsrc((void*)w1, 0, D * DFF * 2, 0x00020000);
    const int sw0 = ((lane & 7) ^ (2 * (grp & 1))) << 4;
#pragma unroll
    for (int u = 0; u < 16; ++u)
      if (XQ == 0) __builtin_amdgcn_raw_ptr_buffer_load_lds(rq, L + u * 1024, 16, (lane >> 3) * (HQ * DH * 2) + (sw0 ^ ((u & 1) * 64)), (128 * (j & 3) + 8 * u) * (HQ * DH * 2) + g * 1024 + (j >> 2) * 128, 0, 2);
      else if (XQ == 1) __builtin_amdgcn_raw_ptr_buffer_load_lds(rq, L + u * 1024, 16, lane * 16, bb * 16384 + u * 1024, 0, 2);
      else if (XQ == 2) __builtin_amdgcn_raw_ptr_buffer_load_lds(rq, L + u * 1024, 16, lane * 16, (16 * j + u) * 8192 + g * 1024, 0, 2);
#pragma unroll
    for (int u = 0; u < 8; ++u)
      if (XQ == 0) __builtin_amdgcn_raw_ptr_buffer_load_lds(r1, L + L_STG + u * 1024, 16, (lane >> 3) * (DFF * 2) + (sw0 ^ ((u & 1) * 64)), (64 * (ff & 7) + 8 * u) * (DFF * 2) + (ff >> 3) * 128, 0, 2);
      else if (XQ < 3) __builtin_amdgcn_raw_ptr_buffer_load_lds(r1, L + L_STG + u * 1024, 16, lane * 16, ff * 8192 + u * 1024, 0, 2);
    PIN4(E); PIN4(xv); PIN4(xr);
    const unsigned tagA = E[0] + 1u, tagB = E[1] + 1u;
    {
      float xf[8];
#pragma unroll
      for (int k = 0; k < 4; ++k) { xf[2 * k] = lo16(xv[k]); xf[2 * k + 1] = hi16(xv[k]); }
      float s = 0.f;
#pragma unroll
      for (int k = 0; k < 8; ++k) s += xf[k];
      const float mean = wave_sum(s) * (1.f / D);
      float v = 0.f;
#pragma unroll
      for (int k = 0; k < 8; ++k) { const float d = xf[k] - mean; v += d * d; }
      const float var = wave_sum(v) * (1.f / D);
      const float mb = rbf(mean), den = rbf(sqrtf(rbf(rbf(var) + 1e-5f)));
      v4u xn;
#pragma unroll
      for (int k = 0; k < 4; ++k) xn[k] = cvtpk(rbf(xf[2 * k] - mb) / den, rbf(xf[2 * k + 1] - mb) / den);
      *(LAS v4u*)(L + L_XN + lane * 16) = xn;
    }
    __builtin_amdgcn_s_waitcnt(0x0F70);
    STAMP(2);
    const f32x2 qp = slice_matvec<4>(L, L + L_XN + (j & 3) * 256, grp, i);
    if (i < 8) __builtin_amdgcn_raw_buffer_store_b128(v4u{__float_as_uint(qp[0]), tagA, __float_as_uint(qp[1]), tagB}, rw, W_Q + bb * 512 + (grp * 8 + i) * 16, 0, 16);
    asm volatile("" ::: "memory");
    const unsigned tq = now32();
    if (lane == 0) *(volatile LAS unsigned*)(L + L_FREE) = 1u;
    STAMP(3);
    const f32x2 hp = slice_matvec<2>(L + L_STG, L + L_XN + (ff & 7) * 128, grp, i);
    if (i < 8 && r32 < 28) __builtin_amdgcn_raw_buffer_store_b128(v4u{__float_as_uint(hp[0]), tagA, __float_as_uint(hp[1]), tagB}, rw, W_H + ff * 512 + (grp * 8 + i) * 16, 0, 16);
    wait_until(tq + QDELAY);
    {
      v4u qq[4][4];
      const int qa = W_Q + (g * 32 + 4 * (lane >> 5)) * 512 + (lane & 31) * 16;
      _Pragma("unroll 1") for (unsigned n = 0; n < SPIN_CAP; ++n) {
        asm volatile("" ::: "memory");
        unsigned bad = 0;
#pragma unroll
        for (int k = 0; k < 4; ++k)
#pragma unroll
          for (int rq4 = 0; rq4 < 4; ++rq4) qq[k][rq4] = __builtin_amdgcn_raw_buffer_load_b128(rw, qa + k * 4096 + rq4 * 512, 0, 16);
#pragma unroll
        for (int k = 0; k < 4; ++k)
#pragma unroll
          for (int rq4 = 0; rq4 < 4; ++rq4) bad |= TAGS_BAD(qq[k][rq4]);
        if (bad == 0) break;
      }
      const int pos = lane & 31, pi = 8 * ((pos >> 1) & 3) + 2 * (pos >> 3) + (pos & 1);
#pragma unroll
      for (int k = 0; k < 4; ++k) {
        const float q0 = (__uint_as_float(qq[k][0][0]) + __uint_as_float(qq[k][1][0])) + (__uint_as_float(qq[k][2][0]) + __uint_as_float(qq[k][3][0]));
        const float q1 = (__uint_as_float(qq[k][0][2]) + __uint_as_float(qq[k][1][2])) + (__uint_as_float(qq[k][2][2]) + __uint_as_float(qq[k][3][2]));
        *(LAS unsigned*)(L + L_QB + k * 256 + (lane >> 5) * 128 + pi * 4) = cvtpk(q0, q1);
      }
      asm volatile("s_waitcnt lgkmcnt(0)" ::: "memory");
      if (lane == 0) *(volatile LAS unsigned*)(L + L_QRDY) = 1u;
    }
    STAMP(4);
    unsigned t1 = 0, tc = 0, sb = 0;
    int ck = 0, c = CK0, cp = 0, tau = TAU16; bool have = false;
#pragma unroll 1
    for (int z = CK0; z <= NTILE - 2; ++z) {
      const unsigned th = 64 + 16 * z;
      const unsigned fv = __builtin_amdgcn_readfirstlane(lds_wait<1>(L_FULL, th));
      const unsigned t = now32() - (((fv - th) * TAU16) >> 8);
      if (have || !PACE) {
        const int lag = (int)(t - sb) - (((z - cp) * tau) >> 4);
        const unsigned lev = !PACE ? 2 : (lag < 3 * BAND / 2) + (lag < BAND / 2) + (lag < -BAND / 2) + (lag < -3 * BAND / 2);
        if (lane == 0) *(volatile LAS unsigned*)(L + L_LEV) = 0x400 + lev;
      }
      if (z == c) {
        tc = t;
        if ((r32 & 15) == 5 && lane < 8) __builtin_amdgcn_raw_buffer_store_b128(v4u{tc, tagA, tc, tagB}, rw, W_T + ck * 2048 + (xcd * 2 + (r32 >> 4)) * 128 + lane * 16, 0, 16);
      }
      if (z == 20) {
        f32x2 h = {0.f, 0.f};
        if (r32 < 28) {
          const int pi = 4 * (ff & 7) + (lane & 3), pos = 8 * ((pi >> 1) & 3) + 2 * (pi >> 3) + (pi & 1);
          v4u hv;
          _Pragma("unroll 1") for (unsigned n = 0; n < SPIN_CAP; ++n) {
            asm volatile("" ::: "memory");
            hv = __builtin_amdgcn_raw_buffer_load_b128(rw, W_H + ((ff & ~7) + ((lane >> 2) & 7)) * 512 + pos * 16, 0, 16);
            if (TAGS_BAD(hv) == 0) break;
          }
          float h0 = lane < 32 ? __uint_as_float(hv[0]) : 0.f, h1 = lane < 32 ? __uint_as_float(hv[2]) : 0.f;
          h0 += dpp<ROR8>(h0); h1 += dpp<ROR8>(h1); h0 += dpp<ROR4>(h0); h1 += dpp<ROR4>(h1);
          h0 = x16_32_sum(h0); h1 = x16_32_sum(h1);
          h = f32x2{fmaxf(rbf(h0), 0.f), fmaxf(rbf(h1), 0.f)};
        }
        if (lane < 4) *(LAS f32x2*)(L + L_OV + 64 + lane * 8) = h;
        asm volatile("s_waitcnt lgkmcnt(0)" ::: "memory");
        if (lane == 0) *(volatile LAS unsigned*)(L + L_HRDY) = 1u;
      }
      if (z == c + CKD) {
        const v4u ra = __builtin_amdgcn_raw_buffer_load_b128(rw, W_T + ck * 2048 + i * 128, 0, 16);
        const bool ok = TAGS_BAD(ra) == 0;
        float sum = ok ? __builtin_amdgcn_fmed3f((float)(int)(ra[0] - tc), -400.f, 400.f) : 0.f, cnt = ok ? 1.f : 0.f;
        sum += dpp<XOR1>(sum); sum += dpp<XOR2>(sum); sum += dpp<XOR7>(sum); sum += dpp<ROR8>(sum);
        cnt += dpp<XOR1>(cnt); cnt += dpp<XOR2>(cnt); cnt += dpp<XOR7>(cnt); cnt += dpp<ROR8>(cnt);
        const unsigned sn = tc + __builtin_amdgcn_readfirstlane((int)(sum * __builtin_amdgcn_rcpf(fmaxf(cnt, 1.f))));
        if (ADAPT && have) tau = (int)(sn - sb) * 16 / CKS - (ADAPT - 1);
        sb = sn; cp = c;
        have = true; ++ck; c += CKS;
      }
    }
    t1 = sb + (((NTILE + 1 - cp) * tau) >> 4) + C1;
    const int p8 = lane >> 3, k8 = lane & 7;
    v4u gm[4], gv[4];
    const int am = W_ML + (g * 32 + p8) * 128 + (k8 & 3) * 16, ao = W_O + (g * 32 + p8) * 4096 + j * 128 + k8 * 16;
    lds_wait<1>(L_FULL, FE - 8);
    wait_until(t1 - MLLEAD);
#pragma unroll
    for (int it = 0; it < 4; ++it) gm[it] = __builtin_amdgcn_raw_buffer_load_b128(rw, am + it * 8 * 128, 0, 16);
    lds_wait<0>(L_FULL, FE);
    { const unsigned t2 = now32() + OWND; wait_until((int)(t2 - t1) > 0 ? t2 : t1); }
    STAMP(12);
#pragma unroll
    for (int it = 0; it < 4; ++it) gv[it] = __builtin_amdgcn_raw_buffer_load_b128(rw, ao + it * 8 * 4096, 0, 16);
    asm volatile("" ::: "memory");
    _Pragma("unroll 1") for (unsigned n = 0; n < SPIN_CAP && (TAGS_BAD(gm[0]) | TAGS_BAD(gm[1]) | TAGS_BAD(gm[2]) | TAGS_BAD(gm[3])) != 0; ++n) {
      asm volatile("" ::: "memory");
#pragma unroll
      for (int it = 0; it < 4; ++it) gm[it] = __builtin_amdgcn_raw_buffer_load_b128(rw, am + it * 8 * 128, 0, 16);
    }
    float M = fmaxf(fmaxf(__uint_as_float(gm[0][0]), __uint_as_float(gm[1][0])), fmaxf(__uint_as_float(gm[2][0]), __uint_as_float(gm[3][0])));
    M = fmaxf(M, dpp<ROR8>(M)); M = x16_32_max(M);
    float sc[4], Ls = 0.f;
#pragma unroll
    for (int it = 0; it < 4; ++it) { sc[it] = __builtin_amdgcn_exp2f((__uint_as_float(gm[it][0]) - M) * LOG2E); Ls += sc[it] * __uint_as_float(gm[it][2]); }
    Ls += dpp<ROR8>(Ls); Ls = x16_32_sum(Ls);
    const float rl = 1.f / Ls;
#pragma unroll
    for (int it = 0; it < 4; ++it) { sc[it] *= rl; PIN4(sc[it]); }
    _Pragma("unroll 1") for (unsigned n = 0; n < SPIN_CAP && (TAGS_BAD(gv[0]) | TAGS_BAD(gv[1]) | TAGS_BAD(gv[2]) | TAGS_BAD(gv[3])) != 0; ++n) {
      asm volatile("" ::: "memory");
#pragma unroll
      for (int it = 0; it < 4; ++it) gv[it] = __builtin_amdgcn_raw_buffer_load_b128(rw, ao + it * 8 * 4096, 0, 16);
    }
    STAMP(7);
    float o0 = (sc[0] * __uint_as_float(gv[0][0]) + sc[1] * __uint_as_float(gv[1][0])) + (sc[2] * __uint_as_float(gv[2][0]) + sc[3] * __uint_as_float(gv[3][0]));
    float o1 = (sc[0] * __uint_as_float(gv[0][2]) + sc[1] * __uint_as_float(gv[1][2])) + (sc[2] * __uint_as_float(gv[2][2]) + sc[3] * __uint_as_float(gv[3][2]));
    o0 += dpp<ROR8>(o0); o1 += dpp<ROR8>(o1);
    o0 = x16_32_sum(o0); o1 = x16_32_sum(o1);
    const unsigned ob = cvtpk(o0, o1);
    if (lane < 8) *(LAS f32x2*)(L + L_OV + (lane & 3) * 16 + (lane >> 2) * 8) = f32x2{lo16(ob), hi16(ob)};
    bar();
    wait_until(now32() + D2);
    const int ap = W_P + lane * 4096 + ((pc * 16) ^ ((lane & 31) << 7));
    _Pragma("unroll 1") for (unsigned n = 0; n < SPIN_CAP; ++n) {
      asm volatile("" ::: "memory");
#pragma unroll
      for (int it = 0; it < 4; ++it) gv[it] = __builtin_amdgcn_raw_buffer_load_b128(rw, ap + it * 64 * 4096, 0, 16);
      if ((TAGS_BAD(gv[0]) | TAGS_BAD(gv[1]) | TAGS_BAD(gv[2]) | TAGS_BAD(gv[3])) == 0) break;
    }
    const float y0 = wave_sum((__uint_as_float(gv[0][0]) + __uint_as_float(gv[1][0])) + (__uint_as_float(gv[2][0]) + __uint_as_float(gv[3][0])));
    const float y1 = wave_sum((__uint_as_float(gv[0][2]) + __uint_as_float(gv[1][2])) + (__uint_as_float(gv[2][2]) + __uint_as_float(gv[3][2])));
    if (lane == 0) {
      *(unsigned*)(out + 2 * pc) = cvtpk(lo16(xr) + y0, hi16(xr) + y1);
      if (b == 0) __builtin_amdgcn_raw_buffer_store_b64(v2u{tagA + 7u, tagB}, rw, W_E, 0, 16);
    }
    STAMP(9); FLUSH();
    return;
  }

  const int cw = wave - 1;
  PIN4(E);
  const unsigned tagA = E[0] + 1u, tagB = E[1] + 1u;
  v4u qf[4];
  f32x4 a0 = {0.f, 0.f, 0.f, 0.f}, a1 = {0.f, 0.f, 0.f, 0.f};
  float m = -INFINITY, l = 0.f;
  const int c4 = i >> 2;
  const int ka0 = i * 256 + ((grp ^ swz(i)) << 4);
  const int va0 = 8192 + (4 * grp + c4) * 256 + ((((i & 3) >> 1) ^ swz(4 * grp + c4)) << 4) + (i & 1) * 8;
  int slot = cw + 1;
  bool first = true;
#pragma unroll 1
  for (int z = slot; z <= NZ; z += 4) {
    LAS char* S = L + slot * SLOT;
    v4u kf[2][4], vf[8], pm[4];
    lds_wait<3>(L_FULL, 64 + 16 * z + 8);
    read_k(S, ka0, kf);
    if (first) {
      lds_wait<1>(L_FULL, 64 + 16 * z + 16);
      if (cw == 0) STAMP(14);
      if (cw == 3) STAMP(15);
      read_v(S, va0, vf);
      asm volatile("s_waitcnt lgkmcnt(0)" ::: "memory");
      if (lane == 0) *(volatile LAS unsigned*)(L + L_FREE + slot * 4) = z + 1;
      lds_wait<2>(L_QRDY, 1);
#pragma unroll
      for (int s = 0; s < 4; ++s) qf[s] = *(LAS v4u*)(L + L_QB + (i & 3) * 256 + s * 64 + grp * 16);
    }
    float xs[8];
    qk_scores(kf, qf, xs);
    const float tm = x16_32_max(fmaxf(fmaxf(fmaxf(xs[0], xs[1]), fmaxf(xs[2], xs[3])), fmaxf(fmaxf(xs[4], xs[5]), fmaxf(xs[6], xs[7]))));
    const float mn = fmaxf(m, tm), alpha = __builtin_amdgcn_exp2f((m - mn) * LOG2E);
    m = mn;
    float pe[8], ps = 0.f;
#pragma unroll
    for (int k = 0; k < 8; ++k) { pe[k] = __builtin_amdgcn_exp2f((xs[k] - mn) * LOG2E); ps += pe[k]; }
    l = l * alpha + ps;
    mask_p(pe, c4, pm);
    a0 *= alpha; a1 *= alpha;
    PIN4(a0); PIN4(a1); PIN4(pm[0]); PIN4(pm[1]); PIN4(pm[2]); PIN4(pm[3]);
    if (!first) {
      lds_wait<1>(L_FULL, 64 + 16 * z + 16);
      read_v(S, va0, vf);
      asm volatile("s_waitcnt lgkmcnt(0)" ::: "memory");
      if (lane == 0) *(volatile LAS unsigned*)(L + L_FREE + slot * 4) = z + 1;
    }
    pv(vf, pm, a0, a1);
    first = false;
    slot += 4; if (slot >= NS) slot -= NS;
  }
  {
    v4u kf[2][4];
    lds_wait<0>(L_FULL, FE - 56 + 8 * cw);
    read_k(L + slot * SLOT, ka0, kf);
    float xs[8];
    qk_scores(kf, qf, xs);
    const float tm = x16_32_max(fmaxf(fmaxf(fmaxf(xs[0], xs[1]), fmaxf(xs[2], xs[3])), fmaxf(fmaxf(xs[4], xs[5]), fmaxf(xs[6], xs[7]))));
    if (lane < 4) *(LAS float*)(L + L_OV + lane * 16 + cw * 4) = fmaxf(m, tm);
    sync4(L, L_SY1, lane);
    const f32x4 mw = *(LAS f32x4*)(L + L_OV + (i & 3) * 16);
    const float M = fmaxf(fmaxf(mw[0], mw[1]), fmaxf(mw[2], mw[3])), alpha = __builtin_amdgcn_exp2f((m - M) * LOG2E);
    float pe[8], ps = 0.f;
#pragma unroll
    for (int k = 0; k < 8; ++k) { pe[k] = __builtin_amdgcn_exp2f((xs[k] - M) * LOG2E); ps += pe[k]; }
    *(LAS v4u*)(L + L_PX + cw * 1024 + lane * 16) = v4u{cvtpk(pe[0], pe[1]), cvtpk(pe[2], pe[3]), cvtpk(pe[4], pe[5]), cvtpk(pe[6], pe[7])};
    *(LAS f32x4*)(L + L_STG + cw * 2048 + lane * 16) = a0 * alpha;
    *(LAS f32x4*)(L + L_STG + cw * 2048 + 1024 + lane * 16) = a1 * alpha;
    *(LAS float*)(L + L_ML + lane * 16 + cw * 4) = l * alpha + ps;
    sync4(L, L_SY2, lane);
    const int e = i >> 3;
    f32x4 acc = {0.f, 0.f, 0.f, 0.f};
    v4u p0[4], p1[4];
#pragma unroll
    for (int w = 0; w < 4; ++w) {
      acc += *(LAS f32x4*)(L + L_STG + w * 2048 + e * 1024 + (grp * 16 + 4 * cw + (i & 3)) * 16);
      const v4u pf = *(LAS v4u*)(L + L_PX + w * 1024 + lane * 16);
      p0[w] = e == 0 ? pf : v4u{0u, 0u, 0u, 0u}; p1[w] = e == 1 ? pf : v4u{0u, 0u, 0u, 0u};
    }
    if (cw == 0) {
      const f32x4 lw = *(LAS f32x4*)(L + L_ML + lane * 16);
      const float ls = x16_32_sum((lw[0] + lw[1]) + (lw[2] + lw[3]));
      if (lane < 4) __builtin_amdgcn_raw_buffer_store_b128(v4u{__float_as_uint(M), tagA, __float_as_uint(ls), tagB}, rw, W_ML + bb * 128 + lane * 16, 0, 16);
    }
    PIN4(acc);
#pragma unroll
    for (int w = 0; w < 4; ++w) { PIN4(p0[w]); PIN4(p1[w]); }
    if (cw == 0) STAMP(5);
    const int vb0 = va0 ^ (cw * 64), vb1 = vb0 ^ 32;
#pragma unroll
    for (int w = 0; w < 4; ++w) {
      LAS char* S = L + ((NZ + 1 + w) % NS) * SLOT;
      lds_wait<0>(L_FULL, FE - 24 + 8 * w);
      if (w == 3 && cw == 1) STAMP(1);
      const v2u u0 = trread(S + vb0), u1 = trread(S + vb0 + 4096), u2 = trread(S + vb1), u3 = trread(S + vb1 + 4096);
      acc = mfma(v4u{u0[0], u0[1], u1[0], u1[1]}, p0[w], acc);
      acc = mfma(v4u{u2[0], u2[1], u3[0], u3[1]}, p1[w], acc);
      if (w < 3) PIN4(acc);
    }
    const bool rh = (i & 4) != 0;
    __builtin_amdgcn_raw_buffer_store_b128(v4u{__float_as_uint(rh ? acc[2] : acc[0]), tagA, __float_as_uint(rh ? acc[3] : acc[1]), tagB}, rw, W_O + bb * 4096 + cw * 1024 + lane * 16, 0, 16);
    if (cw == 0) STAMP(6);
  }
  {
    const int t = cw * 64 + lane;
    int wa4[4];
#pragma unroll
    for (int u = 0; u < 4; ++u) wa4[u] = (t * 4) ^ (swz(4 * u + cw) << 4);
    lds_wait<0>(L_FULL, FE + 24);
    lds_wait<0>(L_HRDY, 1);
    f32x2 wf[16], ya = {0.f, 0.f}, yb = {0.f, 0.f};
#pragma unroll
    for (int u = 0; u < 16; ++u) { const unsigned w = *(LAS unsigned*)(L + SL_WO * SLOT + u * 1024 + wa4[u & 3]); wf[u] = f32x2{lo16(w), hi16(w)}; }
    if (r32 < 28) {
      const f32x4 h0 = *(LAS f32x4*)(L + L_OV + 64), h1 = *(LAS f32x4*)(L + L_OV + 80);
#pragma unroll
      for (int u = 0; u < 4; ++u) {
        const unsigned wa = *(LAS unsigned*)(L + SL_W2 * SLOT + u * 1024 + wa4[u]), wb = *(LAS unsigned*)(L + SL_W2 * SLOT + (4 + u) * 1024 + wa4[u]);
        ya += f32x2{lo16(wa), hi16(wa)} * h0[u]; yb += f32x2{lo16(wb), hi16(wb)} * h1[u];
      }
    }
    int pa = W_P + b * 4096 + ((t * 16) ^ ((b & 31) << 7));
#pragma unroll
    for (int u = 0; u < 16; ++u) PIN4(wf[u]);
    PIN4(ya); PIN4(yb); PIN4(pa);
    bar();
    f32x4 c[4];
#pragma unroll
    for (int u = 0; u < 4; ++u) c[u] = *(LAS f32x4*)(L + L_OV + u * 16);
    asm volatile("" ::: "memory");
#pragma unroll
    for (int u = 0; u < 4; ++u) { ya += wf[4 * u] * c[u][0]; yb += wf[4 * u + 1] * c[u][1]; ya += wf[4 * u + 2] * c[u][2]; yb += wf[4 * u + 3] * c[u][3]; }
    ya += yb;
    __builtin_amdgcn_raw_buffer_store_b128(v4u{__float_as_uint(ya[0]), tagA, __float_as_uint(ya[1]), tagB}, rw, pa, 0, 16);
    if (cw == 0) STAMP(8);
    FLUSH();
  }
}

extern "C" void kernel_launch(void* const* d_in, const int* in_sizes, int n_in, void* d_out, int out_size, void* d_ws, size_t ws_size, hipStream_t stream) {
  (void)hipFuncSetAttribute((const void*)fused_decode, hipFuncAttributeMaxDynamicSharedMemorySize, L_END);
  fused_decode<<<NWG, NTHR, L_END, stream>>>((const u16*)d_in[0], (const u16*)d_in[1], (const u16*)d_in[2], (const u16*)d_in[3], (const u16*)d_in[4],
                                             (const u16*)d_in[6], (const u16*)d_in[7], (u16*)d_out, (char*)d_ws);
}
```

```cpp
#define PROBE 99
#define PMUL 1
#include <hip/hip_runtime.h>
#include <stdint.h>

#define DBG 0
#define WA_ 27
#define WB_ 23
#define WC_ 21
#define WD_ 19
#define WE_ 17
#define LEV0 2
#define BAND 30
#define ADAPT 1
#define TAU16 875
#define CK0 8
#define CKS 16
#define CKD 4
#define ROTM 0
#define OWND 25
#define XQ 0
#define PAT 1
#define ASG 0
#define CODE_KB 16
#define OUTSC 0
#define PACE 1
#define QDELAY 120
#define C1 110
#define D2 40
#define MLLEAD 45
#define MAPB 0

#define LAS __attribute__((address_space(3)))
typedef unsigned v4u __attribute__((ext_vector_type(4)));
typedef unsigned v2u __attribute__((ext_vector_type(2)));
typedef float f32x4 __attribute__((ext_vector_type(4)));
typedef float f32x2 __attribute__((ext_vector_type(2)));
typedef __bf16 bf16x8 __attribute__((ext_vector_type(8)));
typedef __bf16 bf16x2 __attribute__((ext_vector_type(2)));
typedef short s16x4 __attribute__((ext_vector_type(4)));
typedef unsigned short u16;

constexpr int D = 512, HQ = 32, HKV = 8, SK = 65536, DH = 128, DFF = 1792;
constexpr int NWG = 256, NTHR = 384, NTILE = 64;
constexpr int NS = 9, SLOT = 16384;
constexpr int L_XN = NS * SLOT, L_QB = L_XN + 1024, L_STG = L_QB + 1024, L_PX = L_STG + 8192, L_ML = L_PX + 4096, L_OV = L_ML + 1024,
              L_FULL = L_OV + 128, L_FREE = L_FULL + 256, L_QRDY = L_FREE + 64, L_HRDY = L_QRDY + 4, L_SY1 = L_QRDY + 8, L_SY2 = L_QRDY + 12,
              L_LEV = L_FREE + NS * 4, L_END = L_QRDY + 64;
static_assert(L_END <= 163840, "LDS");
constexpr int W_E = 0, W_ML = 4096, W_T = 40960, W_Q = 65536, W_H = W_Q + 131072, W_O = W_H + 131072, W_P = W_O + (1 << 20), W_END = W_P + (1 << 20), W_DBG = 4 << 20;
constexpr unsigned SPIN_CAP = 1u << 16;
constexpr float LOG2E = 1.4426950408889634f, SCALE = 0.08838834764831845f;

__device__ __forceinline__ unsigned cvtpk(float lo, float hi) { return __builtin_bit_cast(unsigned, __builtin_convertvector(f32x2{lo, hi}, bf16x2)); }
__device__ __forceinline__ float lo16(unsigned u) { return __uint_as_float(u << 16); }
__device__ __forceinline__ float hi16(unsigned u) { return __uint_as_float(u & 0xffff0000u); }
__device__ __forceinline__ float rbf(float x) { return lo16(cvtpk(x, x)); }
template <int N> __device__ __forceinline__ void wait_vm() { asm volatile("s_waitcnt vmcnt(%0)" ::"n"(N) : "memory"); }
template <int CTRL> __device__ __forceinline__ float dpp(float x) { return __builtin_bit_cast(float, __builtin_amdgcn_mov_dpp(__builtin_bit_cast(int, x), CTRL, 0xf, 0xf, true)); }
constexpr int XOR1 = 0xB1, XOR2 = 0x4E, XOR7 = 0x141, ROR8 = 0x128, ROR4 = 0x124;
__device__ __forceinline__ float x16_32_max(float x) {
  auto s = __builtin_amdgcn_permlane16_swap(__float_as_uint(x), __float_as_uint(x), false, false);
  x = fmaxf(__uint_as_float(s[0]), __uint_as_float(s[1]));
  auto t = __builtin_amdgcn_permlane32_swap(__float_as_uint(x), __float_as_uint(x), false, false);
  return fmaxf(__uint_as_float(t[0]), __uint_as_float(t[1]));
}
__device__ __forceinline__ float x16_32_sum(float x) {
  auto s = __builtin_amdgcn_permlane16_swap(__float_as_uint(x), __float_as_uint(x), false, false);
  x = __uint_as_float(s[0]) + __uint_as_float(s[1]);
  auto t = __builtin_amdgcn_permlane32_swap(__float_as_uint(x), __float_as_uint(x), false, false);
  return __uint_as_float(t[0]) + __uint_as_float(t[1]);
}
__device__ __forceinline__ float wave_sum(float x) {
  x += dpp<XOR1>(x); x += dpp<XOR2>(x); x += dpp<XOR7>(x); x += dpp<ROR8>(x);
  return x16_32_sum(x);
}
__device__ __forceinline__ f32x4 mfma(v4u a, v4u b, f32x4 c) { return __builtin_amdgcn_mfma_f32_16x16x32_bf16(__builtin_bit_cast(bf16x8, a), __builtin_bit_cast(bf16x8, b), c, 0, 0, 0); }
__device__ __forceinline__ v2u trread(LAS char* p) { return __builtin_bit_cast(v2u, __builtin_amdgcn_ds_read_tr16_b64_v4i16((LAS s16x4*)p)); }
__device__ __forceinline__ void bar() { asm volatile("s_waitcnt lgkmcnt(0)\n\ts_barrier" ::: "memory"); }
template <int SL> __device__ __forceinline__ unsigned lds_wait(int off, unsigned need) {
  unsigned v, n;
  if (SL == 0)
    asm volatile("s_mov_b32 %1, %4\n.Lw%=:\n\tds_read_b32 %0, %2\n\ts_sub_u32 %1, %1, 1\n\ts_waitcnt lgkmcnt(0)\n\tv_cmp_le_u32 vcc, %3, %0\n\ts_cbranch_scc1 .Lx%=\n\ts_cbranch_vccz .Lw%=\n.Lx%=:"
                 : "=&v"(v), "=&s"(n) : "v"(off), "s"(need), "n"(SPIN_CAP) : "memory", "vcc", "scc");
  else
    asm volatile("s_mov_b32 %1, %4\n.Lw%=:\n\tds_read_b32 %0, %2\n\ts_waitcnt lgkmcnt(0)\n\tv_cmp_le_u32 vcc, %3, %0\n\ts_cbranch_vccnz .Lx%=\n\ts_sleep %5\n\ts_sub_u32 %1, %1, 1\n\ts_cbranch_scc0 .Lw%=\n.Lx%=:"
                 : "=&v"(v), "=&s"(n) : "v"(off), "s"(need), "n"(SPIN_CAP), "n"(SL) : "memory", "vcc", "scc");
  return v;
}
#define STAMP(k) do { if ((k) == PROBE) pr_tk = __builtin_amdgcn_s_memrealtime(); \
    if ((k) == 9) { const unsigned long long tgt = tE + 6000 + PMUL * (pr_tk - tE); _Pragma("unroll 1") for (int n_ = 0; n_ < 4000000 && __builtin_amdgcn_s_memrealtime() < tgt; ++n_) __builtin_amdgcn_s_sleep(1); } } while (0)
#define FLUSH() do {} while (0)
#define TAGS_BAD(v) (((v)[1] ^ tagA) | ((v)[3] ^ tagB))
#define PIN4(a) asm volatile("" : "+v"(a))

__device__ __forceinline__ void qk_scores(const v4u (&kf)[2][4], const v4u (&qf)[4], float* xs) {
  f32x4 s0 = {0.f, 0.f, 0.f, 0.f}, s1 = {0.f, 0.f, 0.f, 0.f};
#pragma unroll
  for (int s = 0; s < 4; ++s) { s0 = mfma(kf[0][s], qf[s], s0); s1 = mfma(kf[1][s], qf[s], s1); }
#pragma unroll
  for (int k = 0; k < 4; ++k) {
    const f32x4 sc = k < 2 ? s0 : s1;
    const unsigned r0 = cvtpk(sc[(k & 1) * 2], sc[(k & 1) * 2 + 1]);
    const unsigned r1 = cvtpk(lo16(r0) * SCALE, hi16(r0) * SCALE);
    xs[2 * k] = lo16(r1); xs[2 * k + 1] = hi16(r1);
  }
}
__device__ __forceinline__ void read_k(LAS char* S, int ka0, v4u (&kf)[2][4]) {
#pragma unroll
  for (int s = 0; s < 4; ++s)
#pragma unroll
    for (int u = 0; u < 2; ++u) kf[u][s] = *(LAS v4u*)(S + (ka0 ^ (s * 64)) + u * 4096);
}
__device__ __forceinline__ void read_v(LAS char* S, int va0, v4u (&vf)[8]) {
#pragma unroll
  for (int n = 0; n < 8; ++n) {
    const v2u a = trread(S + (va0 ^ (n * 32))), c = trread(S + (va0 ^ (n * 32)) + 4096);
    vf[n] = v4u{a[0], a[1], c[0], c[1]};
  }
}
__device__ __forceinline__ void mask_p(const float* pe, int c4, v4u (&pm)[4]) {
  const v4u pf = {cvtpk(pe[0], pe[1]), cvtpk(pe[2], pe[3]), cvtpk(pe[4], pe[5]), cvtpk(pe[6], pe[7])};
#pragma unroll
  for (int c = 0; c < 4; ++c) pm[c] = c4 == c ? pf : v4u{0u, 0u, 0u, 0u};
}
__device__ __forceinline__ void pv(const v4u (&vf)[8], const v4u (&pm)[4], f32x4& a0, f32x4& a1) {
#pragma unroll
  for (int c = 0; c < 4; ++c) { a0 = mfma(vf[2 * c], pm[c], a0); a1 = mfma(vf[2 * c + 1], pm[c], a1); }
}
template <int NST>
__device__ __forceinline__ f32x2 slice_matvec(LAS char* W, LAS char* xn, int grp, int i) {
  const int base = (8 * grp + (i >> 2)) * 128 + ((((i & 3) >> 1) ^ (2 * (((i >> 3) & 1) | ((grp & 1) << 1)))) << 4) + (i & 1) * 8;
  f32x4 a[4];
#pragma unroll
  for (int ct = 0; ct < 4; ++ct) a[ct] = f32x4{0.f, 0.f, 0.f, 0.f};
#pragma unroll
  for (int s = 0; s < NST; ++s) {
    const v4u xb = *(LAS v4u*)(xn + s * 64 + grp * 16);
#pragma unroll
    for (int ct = 0; ct < 4; ++ct) {
      const v2u e0 = trread(W + (base ^ (ct * 32)) + s * 4096), e1 = trread(W + (base ^ (ct * 32)) + s * 4096 + 512);
      a[ct] = mfma(v4u{e0[0], e0[1], e1[0], e1[1]}, xb, a[ct]);
    }
  }
  const f32x4 lo = (i & 2) ? a[1] : a[0], hi = (i & 2) ? a[3] : a[2], v = (i & 4) ? hi : lo;
  return (i & 1) ? f32x2{v[2], v[3]} : f32x2{v[0], v[1]};
}

__device__ __forceinline__ unsigned now32() { return (unsigned)__builtin_amdgcn_s_memrealtime(); }
__device__ __forceinline__ void wait_until(unsigned t) {
  unsigned long long c; unsigned n;
  t = __builtin_amdgcn_readfirstlane(t);
  asm volatile("s_mov_b32 %1, %3\n.Lw%=:\n\ts_memrealtime vcc\n\ts_sub_u32 %1, %1, 1\n\ts_cbranch_scc1 .Lx%=\n\ts_waitcnt lgkmcnt(0)\n\ts_sub_u32 vcc_lo, vcc_lo, %2\n\ts_cmp_lt_i32 vcc_lo, 0\n\ts_cbranch_scc1 .Lw%=\n.Lx%=:\n\ts_waitcnt lgkmcnt(0)"
               : "=&s"(c), "=&s"(n) : "s"(t), "n"(SPIN_CAP) : "memory", "vcc", "scc");
}
__device__ __forceinline__ void sync4(LAS char* L, int off, int lane) {
  asm volatile("" ::: "memory");
  if (lane == 0) __hip_atomic_fetch_add((LAS unsigned*)(L + off), 1u, __ATOMIC_RELAXED, __HIP_MEMORY_SCOPE_WORKGROUP);
  lds_wait<0>(off, 4u);
}
__device__ __forceinline__ int swz(int row) { return 2 * ((row & 7) ^ (4 * (row >> 3))) + (row >> 3); }

constexpr int STEP_SH = PAT == 0 ? 0 : PAT == 1 ? 4 : 1, STEP = 4096 << STEP_SH;
__device__ __forceinline__ int tile_off(int j, int n) {
  const int k = n & 15, ph = n >> 4;
  const int in = PAT == 0 ? k << 13 : PAT == 1 ? ((k & 1) << 13) | ((k & 2) << 11) | ((k & 12) << 12) : ((k & 1) << 16) | ((k & 2) << 11) | ((k & 12) << 12);
  return in + (ASG == 0 ? (j << 19) + (((ph + (j & ROTM)) & 3) << 17) : (((j >> 4) * 4 + ph) << 21) + ((j & 15) << 17));
}

#define STR_(x) #x
#define STR(x) STR_(x)
#define GATE(W) "s_waitcnt vmcnt(" STR(W) "-2)\n\tds_write_b32 %[fa], %[ld]\n\tv_add_u32 %[ld], 2, %[ld]\n\t"
#define LD2A(so) "buffer_load_dwordx4 %[v0], " so " offen nt lds\n\tbuffer_load_dwordx4 %[v1], " so " offen offset:1024 nt lds\n\t"
#define LD2B(so) "buffer_load_dwordx4 %[v2], " so " offen offset:2048 nt lds\n\tbuffer_load_dwordx4 %[v3], " so " offen offset:3072 nt lds\n\t"
#define FILL(W) \
  "s_lshl_b32 s53, s52, 4\n\ts_mov_b32 m0, s48\n\ts_add_u32 s53, s53, 66-" STR(W) "\n\tv_mov_b32 %[ld], s53\n\t" \
  GATE(W) LD2A("s[40:43], 0") \
  "ds_read_b32 %[fr], %[fra]\n\ts_add_u32 s52, s52, 1\n\ts_and_b32 s54, s52, 63\n\t" \
  GATE(W) LD2B("s[40:43], 0") \
  "s_add_u32 m0, s48, s63\n\t" \
  GATE(W) LD2A("s[40:43], s50") \
  "v_readlane_b32 s61, %[tp], s54\n\t" \
  GATE(W) LD2B("s[40:43], s50") \
  "s_mov_b32 m0, s49\n\t" \
  GATE(W) LD2A("s[44:47], 0") \
  "v_readlane_b32 s40, %[ta0], s54\n\tv_readlane_b32 s41, %[ta1], s54\n\t" \
  "s_and_b32 s48, s61, 0xff\n\ts_lshl_b32 s48, s48, 10\n\ts_bfe_u32 s50, s61, 0x30018\n\ts_lshl_b32 s50, s63, s50\n\ts_add_u32 s42, s50, s63\n\t" \
  GATE(W) LD2B("s[44:47], 0") \
  "s_add_u32 m0, s49, s63\n\ts_bfe_u32 s60, s61, 0x80010\n\ts_sub_u32 s60, s60, 16\n\ts_waitcnt lgkmcnt(2)\n\tv_readlane_b32 s53, %[fr], 9\n\tv_cmp_gt_i32 vcc, s60, %[fr]\n\t" \
  GATE(W) LD2A("s[44:47], s51") \
  "s_sub_u32 s53, s53, 0x400\n\ts_min_u32 s53, s53, 4\n\ts_mul_i32 s53, s53, s64\n\t" \
  "s_cmpk_eq_u32 s52, 67\n\ts_cselect_b32 s53, s66, s53\n\t" \
  "s_add_u32 s56, s58, s53\n\ts_addc_u32 s57, s59, 0\n\t" \
  GATE(W) LD2B("s[44:47], s51") \
  "s_bfe_u32 s49, s61, 0x80008\n\ts_lshl_b32 s49, s49, 10\n\tv_readlane_b32 s44, %[tb0], s54\n\tv_readlane_b32 s45, %[tb1], s54\n\t" \
  "s_mov_b32 s51, s50\n\ts_bitcmp1_b32 s61, 27\n\ts_cselect_b32 s46, 0, s42\n\t" \
  "s_cbranch_vccnz .Lstall%=\n\ts_setpc_b64 s[56:57]\n"

__global__ void __launch_bounds__(NTHR) fused_decode(const u16* __restrict__ x, const u16* __restrict__ Kc, const u16* __restrict__ Vc, const u16* __restrict__ wq,
                                                     const u16* __restrict__ wo, const u16* __restrict__ w1, const u16* __restrict__ w2, u16* __restrict__ out, char* ws) {
  extern __shared__ __attribute__((aligned(16))) char lds_raw[];
  unsigned long long tE; asm volatile("s_memrealtime %0\n\ts_waitcnt lgkmcnt(0)" : "=s"(tE) :: "memory");
  unsigned long long pr_tk = tE; unsigned stv = 0;
  LAS char* L = (LAS char*)lds_raw;
  const int b = blockIdx.x, lane = threadIdx.x & 63, wave = __builtin_amdgcn_readfirstlane(threadIdx.x >> 6);
  const int xcd = b & 7, r32 = b >> 3;
  if (CODE_KB) {
    unsigned long long pc; asm volatile("s_getpc_b64 %0" : "=s"(pc));
    auto rc = __builtin_amdgcn_make_buffer_rsrc((void*)(pc & ~255ull), 0, CODE_KB * 1024, 0x00020000);
    __builtin_amdgcn_raw_ptr_buffer_load_lds(rc, L + L_PX, 16, r32 * 1024 + threadIdx.x * 16 + (threadIdx.x >> 6) * 31 * 1024, 0, 0, 0);
  }
  asm volatile("ds_write_b32 %0, %1\n\tds_write_b32 %0, %2 offset:128" ::"v"(L_FULL + lane * 4), "v"(0u), "v"(lane < 32 || lane >= 32 + NS + 1 ? 0 : lane == 32 + NS ? 0x400 + LEV0 : lane - 32 - (NS - 1)));
  const int g = xcd, j = r32;
  const int bb = g * 32 + j;
  const int grp = lane >> 4, i = lane & 15;
  int vo[4];
#pragma unroll
  for (int u = 0; u < 4; ++u) vo[u] = grp * 256 + ((i ^ swz(4 * u + grp)) << 4);
  {
    auto rk = __builtin_amdgcn_make_buffer_rsrc((void*)Kc, 0, HKV * SK * DH * 2, 0x00020000);
    auto rv = __builtin_amdgcn_make_buffer_rsrc((void*)Vc, 0, HKV * SK * DH * 2, 0x00020000);
    const int s1 = (g << 24) + tile_off(j, 0), s2 = (g << 24) + tile_off(j, 1);
#define LD4(m, r, so) "s_mov_b32 m0, " m "\n\ts_nop 0\n\t" LD2A(r ", " so) LD2B(r ", " so)
    asm volatile("s_waitcnt lgkmcnt(0)\n\ts_barrier\n\ts_cmp_lg_u32 %[wv], 0\n\ts_cbranch_scc1 .Lskip%=\n\ts_setprio 3\n\t"
                 LD4("0x4000", "%[rk]", "%[s1]") LD4("0x5000", "%[rk]", "%[s1b]") LD4("0x6000", "%[rv]", "%[s1]") LD4("0x7000", "%[rv]", "%[s1b]")
                 LD4("0x8000", "%[rk]", "%[s2]") LD4("0x9000", "%[rk]", "%[s2b]") LD4("0xa000", "%[rv]", "%[s2]") LD4("0xb000", "%[rv]", "%[s2b]")
                 "\n.Lskip%=:"
                 :: [wv] "s"(wave), [rk] "s"(rk), [rv] "s"(rv), [s1] "s"(s1), [s1b] "s"(s1 + STEP), [s2] "s"(s2), [s2b] "s"(s2 + STEP),
                    [v0] "v"(vo[0]), [v1] "v"(vo[1]), [v2] "v"(vo[2]), [v3] "v"(vo[3]), "s"(x), "s"(wq), "s"(wo), "s"(w1), "s"(w2), "s"(out), "s"(ws) : "memory", "scc");
  }
  const int ff = xcd * 28 + (r32 < 28 ? r32 : 27);
  constexpr int SL_WO = (NTILE + 1) % NS, SL_W2 = (NTILE + 2) % NS;
  constexpr int NZ = NTILE - 4;

  if (wave == 0) {
    const unsigned long long kb = (unsigned long long)Kc + (g << 24), vb = (unsigned long long)Vc + (g << 24);
    const int z = lane < 3 ? 64 + lane : lane;
    const bool last4 = z > NZ;
    const int t0 = last4 ? NZ + 1 + 2 * ((z - NZ - 1) & 1) : z, t1_ = last4 ? t0 + 1 : z;
    const bool av = z > NZ + 2, bv = !last4 || av;
    unsigned long long ta = (av ? vb : kb) + tile_off(j, t0 - 1), tb = (bv ? vb : kb) + tile_off(j, t1_ - 1);
    unsigned tp = ((t0 % NS) * 16 + (av ? 8 : 0)) | (((t1_ % NS) * 16 + (bv ? 8 : 0)) << 8) | (t1_ << 16) | (STEP_SH << 24);
    if (lane == 1) {
      ta = (unsigned long long)wo + ((4 * g) * 128 + 32 * (j >> 3) + 16 * (j & 1) + 4 * ((j & 7) >> 1)) * 1024; tb = ta + 256 * 1024;
      tp = (SL_WO * 16) | ((SL_WO * 16 + 8) << 8) | ((NTILE + 1) << 16) | (5 << 24);
    }
    if (lane == 2) {
      ta = tb = (unsigned long long)w2 + ff * 8192;
      tp = (SL_W2 * 16) | ((SL_W2 * 16 + 8) << 8) | ((NTILE + 2) << 16) | (1 << 27);
    }
    int ld, fr;
    static_assert(WA_ <= 32 && WB_ <= 32 && WC_ <= 32 && NS == 9 && NTILE == 64, "loader constants");
    asm volatile(
        "s_nop 1\n\tv_readlane_b32 s40, %[ta0], 3\n\tv_readlane_b32 s41, %[ta1], 3\n\tv_readlane_b32 s44, %[tb0], 3\n\tv_readlane_b32 s45, %[tb1], 3\n\t"
        "s_mov_b32 s42, %[stp]+0x1000\n\ts_mov_b32 s43, 0x20000\n\ts_mov_b32 s46, s42\n\ts_mov_b32 s47, s43\n\t"
        "s_mov_b32 s48, 0xc000\n\ts_mov_b32 s49, 0xe000\n\ts_mov_b32 s50, %[stp]\n\ts_mov_b32 s51, s50\n\ts_mov_b32 s52, 3\n\t"
        "s_movk_i32 s63, 0x1000\n\ts_mov_b32 s64, .Lv1%=-.Lv0%=\n\ts_mov_b32 s66, .Lexit%=-.Lv0%=\n\t"
        "s_getpc_b64 s[58:59]\n"
        ".Lv0%=:\n\t" FILL(WA_) ".Lv1%=:\n\t" FILL(WB_) FILL(WC_) FILL(WD_) FILL(WE_)
        ".Lstall%=:\n\ts_mov_b32 s53, %[cap]\n"
        ".Lst%=:\n\tds_read_b32 %[fr], %[fra]\n\ts_sub_u32 s53, s53, 1\n\ts_waitcnt lgkmcnt(0)\n\tv_cmp_gt_i32 vcc, s60, %[fr]\n\ts_cbranch_scc1 .Lsx%=\n\ts_cbranch_vccnz .Lst%=\n"
        ".Lsx%=:\n\ts_setpc_b64 s[56:57]\n"
        ".Lexit%=:\n\tv_mov_b32 %[ld], %[fin]\n\ts_waitcnt vmcnt(0)\n\tds_write_b32 %[fa], %[ld]\n\ts_waitcnt lgkmcnt(0)"
        : [ld] "=&v"(ld), [fr] "=&v"(fr)
        : [ta0] "v"((unsigned)ta), [ta1] "v"((unsigned)(ta >> 32) & 0xffff), [tb0] "v"((unsigned)tb), [tb1] "v"((unsigned)(tb >> 32) & 0xffff), [tp] "v"(tp),
          [fa] "v"(L_FULL + lane * 4), [fra] "v"(L_FREE + (lane <= NS ? lane : 0) * 4), [v0] "v"(vo[0]), [v1] "v"(vo[1]), [v2] "v"(vo[2]), [v3] "v"(vo[3]),
          [cap] "n"(SPIN_CAP), [stp] "n"(STEP), [fin] "n"(64 + 16 * (NTILE + 3))
        : "memory", "vcc", "scc", "s40", "s41", "s42", "s43", "s44", "s45", "s46", "s47", "s48", "s49", "s50", "s51", "s52", "s53", "s54", "s55", "s56", "s57", "s58", "s59",
          "s60", "s61", "s62", "s63", "s64", "s65", "s66");
    STAMP(10); FLUSH();
    return;
  }
  constexpr unsigned FE = 64 + 16 * (NTILE + 1);
  auto rw = __builtin_amdgcn_make_buffer_rsrc((void*)ws, 0, W_END, 0x00020000);
  v2u E = __builtin_amdgcn_raw_buffer_load_b64(rw, W_E, 0, 16);

  if (wave == 5) {
    STAMP(0);
    v4u xv = *(const v4u*)(x + lane * 8);
    const int pc = xcd * 32 + r32;
    unsigned xr = *(const unsigned*)(x + 2 * pc);
    auto rq = __builtin_amdgcn_make_buffer_rsrc((void*)wq, 0, D * HQ * DH * 2, 0x00020000);
    auto r1 = __builtin_amdgcn_make_buffer_rsrc((void*)w1, 0, D * DFF * 2, 0x00020000);
    const int sw0 = ((lane & 7) ^ (2 * (grp & 1))) << 4;
#pragma unroll
    for (int u = 0; u < 16; ++u)
      if (XQ == 0) __builtin_amdgcn_raw_ptr_buffer_load_lds(rq, L + u * 1024, 16, (lane >> 3) * (HQ * DH * 2) + (sw0 ^ ((u & 1) * 64)), (128 * (j & 3) + 8 * u) * (HQ * DH * 2) + g * 1024 + (j >> 2) * 128, 0, 2);
      else if (XQ == 1) __builtin_amdgcn_raw_ptr_buffer_load_lds(rq, L + u * 1024, 16, lane * 16, bb * 16384 + u * 1024, 0, 2);
      else if (XQ == 2) __builtin_amdgcn_raw_ptr_buffer_load_lds(rq, L + u * 1024, 16, lane * 16, (16 * j + u) * 8192 + g * 1024, 0, 2);
#pragma unroll
    for (int u = 0; u < 8; ++u)
      if (XQ == 0) __builtin_amdgcn_raw_ptr_buffer_load_lds(r1, L + L_STG + u * 1024, 16, (lane >> 3) * (DFF * 2) + (sw0 ^ ((u & 1) * 64)), (64 * (ff & 7) + 8 * u) * (DFF * 2) + (ff >> 3) * 128, 0, 2);
      else if (XQ < 3) __builtin_amdgcn_raw_ptr_buffer_load_lds(r1, L + L_STG + u * 1024, 16, lane * 16, ff * 8192 + u * 1024, 0, 2);
    PIN4(E); PIN4(xv); PIN4(xr);
    const unsigned tagA = E[0] + 1u, tagB = E[1] + 1u;
    {
      float xf[8];
#pragma unroll
      for (int k = 0; k < 4; ++k) { xf[2 * k] = lo16(xv[k]); xf[2 * k + 1] = hi16(xv[k]); }
      float s = 0.f;
#pragma unroll
      for (int k = 0; k < 8; ++k) s += xf[k];
      const float mean = wave_sum(s) * (1.f / D);
      float v = 0.f;
#pragma unroll
      for (int k = 0; k < 8; ++k) { const float d = xf[k] - mean; v += d * d; }
      const float var = wave_sum(v) * (1.f / D);
      const float mb = rbf(mean), den = rbf(sqrtf(rbf(rbf(var) + 1e-5f)));
      v4u xn;
#pragma unroll
      for (int k = 0; k < 4; ++k) xn[k] = cvtpk(rbf(xf[2 * k] - mb) / den, rbf(xf[2 * k + 1] - mb) / den);
      *(LAS v4u*)(L + L_XN + lane * 16) = xn;
    }
    __builtin_amdgcn_s_waitcnt(0x0F70);
    STAMP(2);
    const f32x2 qp = slice_matvec<4>(L, L + L_XN + (j & 3) * 256, grp, i);
    if (i < 8) __builtin_amdgcn_raw_buffer_store_b128(v4u{__float_as_uint(qp[0]), tagA, __float_as_uint(qp[1]), tagB}, rw, W_Q + bb * 512 + (grp * 8 + i) * 16, 0, 16);
    asm volatile("" ::: "memory");
    const unsigned tq = now32();
    if (lane == 0) *(volatile LAS unsigned*)(L + L_FREE) = 1u;
    STAMP(3);
    const f32x2 hp = slice_matvec<2>(L + L_STG, L + L_XN + (ff & 7) * 128, grp, i);
    if (i < 8 && r32 < 28) __builtin_amdgcn_raw_buffer_store_b128(v4u{__float_as_uint(hp[0]), tagA, __float_as_uint(hp[1]), tagB}, rw, W_H + ff * 512 + (grp * 8 + i) * 16, 0, 16);
    wait_until(tq + QDELAY);
    {
      v4u qq[4][4];
      const int qa = W_Q + (g * 32 + 4 * (lane >> 5)) * 512 + (lane & 31) * 16;
      _Pragma("unroll 1") for (unsigned n = 0; n < SPIN_CAP; ++n) {
        asm volatile("" ::: "memory");
        unsigned bad = 0;
#pragma unroll
        for (int k = 0; k < 4; ++k)
#pragma unroll
          for (int rq4 = 0; rq4 < 4; ++rq4) qq[k][rq4] = __builtin_amdgcn_raw_buffer_load_b128(rw, qa + k * 4096 + rq4 * 512, 0, 16);
#pragma unroll
        for (int k = 0; k < 4; ++k)
#pragma unroll
          for (int rq4 = 0; rq4 < 4; ++rq4) bad |= TAGS_BAD(qq[k][rq4]);
        if (bad == 0) break;
      }
      const int pos = lane & 31, pi = 8 * ((pos >> 1) & 3) + 2 * (pos >> 3) + (pos & 1);
#pragma unroll
      for (int k = 0; k < 4; ++k) {
        const float q0 = (__uint_as_float(qq[k][0][0]) + __uint_as_float(qq[k][1][0])) + (__uint_as_float(qq[k][2][0]) + __uint_as_float(qq[k][3][0]));
        const float q1 = (__uint_as_float(qq[k][0][2]) + __uint_as_float(qq[k][1][2])) + (__uint_as_float(qq[k][2][2]) + __uint_as_float(qq[k][3][2]));
        *(LAS unsigned*)(L + L_QB + k * 256 + (lane >> 5) * 128 + pi * 4) = cvtpk(q0, q1);
      }
      asm volatile("s_waitcnt lgkmcnt(0)" ::: "memory");
      if (lane == 0) *(volatile LAS unsigned*)(L + L_QRDY) = 1u;
    }
    STAMP(4);
    unsigned t1 = 0, tc = 0, sb = 0;
    int ck = 0, c = CK0, cp = 0, tau = TAU16; bool have = false;
#pragma unroll 1
    for (int z = CK0; z <= NTILE - 2; ++z) {
      const unsigned th = 64 + 16 * z;
      const unsigned fv = __builtin_amdgcn_readfirstlane(lds_wait<1>(L_FULL, th));
      const unsigned t = now32() - (((fv - th) * TAU16) >> 8);
      if ((PROBE == 13 && z == 10) || (PROBE == 11 && z == 62)) pr_tk = tE + (int)(t - (unsigned)tE);
      if (have || !PACE) {
        const int lag = (int)(t - sb) - (((z - cp) * tau) >> 4);
        const unsigned lev = !PACE ? 2 : (lag < 3 * BAND / 2) + (lag < BAND / 2) + (lag < -BAND / 2) + (lag < -3 * BAND / 2);
        if (lane == 0) *(volatile LAS unsigned*)(L + L_LEV) = 0x400 + lev;
      }
      if (z == c) {
        tc = t;
        if ((r32 & 15) == 5 && lane < 8) __builtin_amdgcn_raw_buffer_store_b128(v4u{tc, tagA, tc, tagB}, rw, W_T + ck * 2048 + (xcd * 2 + (r32 >> 4)) * 128 + lane * 16, 0, 16);
      }
      if (z == 20) {
        f32x2 h = {0.f, 0.f};
        if (r32 < 28) {
          const int pi = 4 * (ff & 7) + (lane & 3), pos = 8 * ((pi >> 1) & 3) + 2 * (pi >> 3) + (pi & 1);
          v4u hv;
          _Pragma("unroll 1") for (unsigned n = 0; n < SPIN_CAP; ++n) {
            asm volatile("" ::: "memory");
            hv = __builtin_amdgcn_raw_buffer_load_b128(rw, W_H + ((ff & ~7) + ((lane >> 2) & 7)) * 512 + pos * 16, 0, 16);
            if (TAGS_BAD(hv) == 0) break;
          }
          float h0 = lane < 32 ? __uint_as_float(hv[0]) : 0.f, h1 = lane < 32 ? __uint_as_float(hv[2]) : 0.f;
          h0 += dpp<ROR8>(h0); h1 += dpp<ROR8>(h1); h0 += dpp<ROR4>(h0); h1 += dpp<ROR4>(h1);
          h0 = x16_32_sum(h0); h1 = x16_32_sum(h1);
          h = f32x2{fmaxf(rbf(h0), 0.f), fmaxf(rbf(h1), 0.f)};
        }
        if (lane < 4) *(LAS f32x2*)(L + L_OV + 64 + lane * 8) = h;
        asm volatile("s_waitcnt lgkmcnt(0)" ::: "memory");
        if (lane == 0) *(volatile LAS unsigned*)(L + L_HRDY) = 1u;
      }
      if (z == c + CKD) {
        const v4u ra = __builtin_amdgcn_raw_buffer_load_b128(rw, W_T + ck * 2048 + i * 128, 0, 16);
        const bool ok = TAGS_BAD(ra) == 0;
        float sum = ok ? __builtin_amdgcn_fmed3f((float)(int)(ra[0] - tc), -400.f, 400.f) : 0.f, cnt = ok ? 1.f : 0.f;
        sum += dpp<XOR1>(sum); sum += dpp<XOR2>(sum); sum += dpp<XOR7>(sum); sum += dpp<ROR8>(sum);
        cnt += dpp<XOR1>(cnt); cnt += dpp<XOR2>(cnt); cnt += dpp<XOR7>(cnt); cnt += dpp<ROR8>(cnt);
        const unsigned sn = tc + __builtin_amdgcn_readfirstlane((int)(sum * __builtin_amdgcn_rcpf(fmaxf(cnt, 1.f))));
        if (ADAPT && have) tau = (int)(sn - sb) * 16 / CKS - (ADAPT - 1);
        sb = sn; cp = c;
        have = true; ++ck; c += CKS;
      }
    }
    t1 = sb + (((NTILE + 1 - cp) * tau) >> 4) + C1;
    const int p8 = lane >> 3, k8 = lane & 7;
    v4u gm[4], gv[4];
    const int am = W_ML + (g * 32 + p8) * 128 + (k8 & 3) * 16, ao = W_O + (g * 32 + p8) * 4096 + j * 128 + k8 * 16;
    lds_wait<1>(L_FULL, FE - 8);
    wait_until(t1 - MLLEAD);
#pragma unroll
    for (int it = 0; it < 4; ++it) gm[it] = __builtin_amdgcn_raw_buffer_load_b128(rw, am + it * 8 * 128, 0, 16);
    lds_wait<0>(L_FULL, FE);
    { const unsigned t2 = now32() + OWND; wait_until((int)(t2 - t1) > 0 ? t2 : t1); }
    STAMP(12);
#pragma unroll
    for (int it = 0; it < 4; ++it) gv[it] = __builtin_amdgcn_raw_buffer_load_b128(rw, ao + it * 8 * 4096, 0, 16);
    asm volatile("" ::: "memory");
    _Pragma("unroll 1") for (unsigned n = 0; n < SPIN_CAP && (TAGS_BAD(gm[0]) | TAGS_BAD(gm[1]) | TAGS_BAD(gm[2]) | TAGS_BAD(gm[3])) != 0; ++n) {
      asm volatile("" ::: "memory");
#pragma unroll
      for (int it = 0; it < 4; ++it) gm[it] = __builtin_amdgcn_raw_buffer_load_b128(rw, am + it * 8 * 128, 0, 16);
    }
    float M = fmaxf(fmaxf(__uint_as_float(gm[0][0]), __uint_as_float(gm[1][0])), fmaxf(__uint_as_float(gm[2][0]), __uint_as_float(gm[3][0])));
    M = fmaxf(M, dpp<ROR8>(M)); M = x16_32_max(M);
    float sc[4], Ls = 0.f;
#pragma unroll
    for (int it = 0; it < 4; ++it) { sc[it] = __builtin_amdgcn_exp2f((__uint_as_float(gm[it][0]) - M) * LOG2E); Ls += sc[it] * __uint_as_float(gm[it][2]); }
    Ls += dpp<ROR8>(Ls); Ls = x16_32_sum(Ls);
    const float rl = 1.f / Ls;
#pragma unroll
    for (int it = 0; it < 4; ++it) { sc[it] *= rl; PIN4(sc[it]); }
    _Pragma("unroll 1") for (unsigned n = 0; n < SPIN_CAP && (TAGS_BAD(gv[0]) | TAGS_BAD(gv[1]) | TAGS_BAD(gv[2]) | TAGS_BAD(gv[3])) != 0; ++n) {
      asm volatile("" ::: "memory");
#pragma unroll
      for (int it = 0; it < 4; ++it) gv[it] = __builtin_amdgcn_raw_buffer_load_b128(rw, ao + it * 8 * 4096, 0, 16);
    }
    STAMP(7);
    float o0 = (sc[0] * __uint_as_float(gv[0][0]) + sc[1] * __uint_as_float(gv[1][0])) + (sc[2] * __uint_as_float(gv[2][0]) + sc[3] * __uint_as_float(gv[3][0]));
    float o1 = (sc[0] * __uint_as_float(gv[0][2]) + sc[1] * __uint_as_float(gv[1][2])) + (sc[2] * __uint_as_float(gv[2][2]) + sc[3] * __uint_as_float(gv[3][2]));
    o0 += dpp<ROR8>(o0); o1 += dpp<ROR8>(o1);
    o0 = x16_32_sum(o0); o1 = x16_32_sum(o1);
    const unsigned ob = cvtpk(o0, o1);
    if (lane < 8) *(LAS f32x2*)(L + L_OV + (lane & 3) * 16 + (lane >> 2) * 8) = f32x2{lo16(ob), hi16(ob)};
    bar();
    wait_until(now32() + D2);
    const int ap = W_P + lane * 4096 + ((pc * 16) ^ ((lane & 31) << 7));
    _Pragma("unroll 1") for (unsigned n = 0; n < SPIN_CAP; ++n) {
      asm volatile("" ::: "memory");
#pragma unroll
      for (int it = 0; it < 4; ++it) gv[it] = __builtin_amdgcn_raw_buffer_load_b128(rw, ap + it * 64 * 4096, 0, 16);
      if ((TAGS_BAD(gv[0]) | TAGS_BAD(gv[1]) | TAGS_BAD(gv[2]) | TAGS_BAD(gv[3])) == 0) break;
    }
    const float y0 = wave_sum((__uint_as_float(gv[0][0]) + __uint_as_float(gv[1][0])) + (__uint_as_float(gv[2][0]) + __uint_as_float(gv[3][0])));
    const float y1 = wave_sum((__uint_as_float(gv[0][2]) + __uint_as_float(gv[1][2])) + (__uint_as_float(gv[2][2]) + __uint_as_float(gv[3][2])));
    if (lane == 0) {
      if (OUTSC) __hip_atomic_store((unsigned*)(out + 2 * pc), cvtpk(lo16(xr) + y0, hi16(xr) + y1), __ATOMIC_RELAXED, __HIP_MEMORY_SCOPE_AGENT);
      else *(unsigned*)(out + 2 * pc) = cvtpk(lo16(xr) + y0, hi16(xr) + y1);
      if (b == 0) __builtin_amdgcn_raw_buffer_store_b64(v2u{tagA + 7u, tagB}, rw, W_E, 0, 16);
    }
    STAMP(9); FLUSH();
    return;
  }

  const int cw = wave - 1;
  PIN4(E);
  const unsigned tagA = E[0] + 1u, tagB = E[1] + 1u;
  v4u qf[4];
  f32x4 a0 = {0.f, 0.f, 0.f, 0.f}, a1 = {0.f, 0.f, 0.f, 0.f};
  float m = -INFINITY, l = 0.f;
  const int c4 = i >> 2;
  const int ka0 = i * 256 + ((grp ^ swz(i)) << 4);
  const int va0 = 8192 + (4 * grp + c4) * 256 + ((((i & 3) >> 1) ^ swz(4 * grp + c4)) << 4) + (i & 1) * 8;
  int slot = cw + 1;
  bool first = true;
#pragma unroll 1
  for (int z = slot; z <= NZ; z += 4) {
    LAS char* S = L + slot * SLOT;
    v4u kf[2][4], vf[8], pm[4];
    lds_wait<3>(L_FULL, 64 + 16 * z + 8);
    read_k(S, ka0, kf);
    if (first) {
      lds_wait<1>(L_FULL, 64 + 16 * z + 16);
      if (cw == 0) STAMP(14);
      if (cw == 3) STAMP(15);
      read_v(S, va0, vf);
      asm volatile("s_waitcnt lgkmcnt(0)" ::: "memory");
      if (lane == 0) *(volatile LAS unsigned*)(L + L_FREE + slot * 4) = z + 1;
      lds_wait<2>(L_QRDY, 1);
#pragma unroll
      for (int s = 0; s < 4; ++s) qf[s] = *(LAS v4u*)(L + L_QB + (i & 3) * 256 + s * 64 + grp * 16);
    }
    float xs[8];
    qk_scores(kf, qf, xs);
    const float tm = x16_32_max(fmaxf(fmaxf(fmaxf(xs[0], xs[1]), fmaxf(xs[2], xs[3])), fmaxf(fmaxf(xs[4], xs[5]), fmaxf(xs[6], xs[7]))));
    const float mn = fmaxf(m, tm), alpha = __builtin_amdgcn_exp2f((m - mn) * LOG2E);
    m = mn;
    float pe[8], ps = 0.f;
#pragma unroll
    for (int k = 0; k < 8; ++k) { pe[k] = __builtin_amdgcn_exp2f((xs[k] - mn) * LOG2E); ps += pe[k]; }
    l = l * alpha + ps;
    mask_p(pe, c4, pm);
    a0 *= alpha; a1 *= alpha;
    PIN4(a0); PIN4(a1); PIN4(pm[0]); PIN4(pm[1]); PIN4(pm[2]); PIN4(pm[3]);
    if (!first) {
      lds_wait<1>(L_FULL, 64 + 16 * z + 16);
      read_v(S, va0, vf);
      asm volatile("s_waitcnt lgkmcnt(0)" ::: "memory");
      if (lane == 0) *(volatile LAS unsigned*)(L + L_FREE + slot * 4) = z + 1;
    }
    pv(vf, pm, a0, a1);
    first = false;
    slot += 4; if (slot >= NS) slot -= NS;
  }
  {
    v4u kf[2][4];
    lds_wait<0>(L_FULL, FE - 56 + 8 * cw);
    read_k(L + slot * SLOT, ka0, kf);
    float xs[8];
    qk_scores(kf, qf, xs);
    const float tm = x16_32_max(fmaxf(fmaxf(fmaxf(xs[0], xs[1]), fmaxf(xs[2], xs[3])), fmaxf(fmaxf(xs[4], xs[5]), fmaxf(xs[6], xs[7]))));
    if (lane < 4) *(LAS float*)(L + L_OV + lane * 16 + cw * 4) = fmaxf(m, tm);
    sync4(L, L_SY1, lane);
    const f32x4 mw = *(LAS f32x4*)(L + L_OV + (i & 3) * 16);
    const float M = fmaxf(fmaxf(mw[0], mw[1]), fmaxf(mw[2], mw[3])), alpha = __builtin_amdgcn_exp2f((m - M) * LOG2E);
    float pe[8], ps = 0.f;
#pragma unroll
    for (int k = 0; k < 8; ++k) { pe[k] = __builtin_amdgcn_exp2f((xs[k] - M) * LOG2E); ps += pe[k]; }
    *(LAS v4u*)(L + L_PX + cw * 1024 + lane * 16) = v4u{cvtpk(pe[0], pe[1]), cvtpk(pe[2], pe[3]), cvtpk(pe[4], pe[5]), cvtpk(pe[6], pe[7])};
    *(LAS f32x4*)(L + L_STG + cw * 2048 + lane * 16) = a0 * alpha;
    *(LAS f32x4*)(L + L_STG + cw * 2048 + 1024 + lane * 16) = a1 * alpha;
    *(LAS float*)(L + L_ML + lane * 16 + cw * 4) = l * alpha + ps;
    sync4(L, L_SY2, lane);
    const int e = i >> 3;
    f32x4 acc = {0.f, 0.f, 0.f, 0.f};
    v4u p0[4], p1[4];
#pragma unroll
    for (int w = 0; w < 4; ++w) {
      acc += *(LAS f32x4*)(L + L_STG + w * 2048 + e * 1024 + (grp * 16 + 4 * cw + (i & 3)) * 16);
      const v4u pf = *(LAS v4u*)(L + L_PX + w * 1024 + lane * 16);
      p0[w] = e == 0 ? pf : v4u{0u, 0u, 0u, 0u}; p1[w] = e == 1 ? pf : v4u{0u, 0u, 0u, 0u};
    }
    if (cw == 0) {
      const f32x4 lw = *(LAS f32x4*)(L + L_ML + lane * 16);
      const float ls = x16_32_sum((lw[0] + lw[1]) + (lw[2] + lw[3]));
      if (lane < 4) __builtin_amdgcn_raw_buffer_store_b128(v4u{__float_as_uint(M), tagA, __float_as_uint(ls), tagB}, rw, W_ML + bb * 128 + lane * 16, 0, 16);
    }
    PIN4(acc);
#pragma unroll
    for (int w = 0; w < 4; ++w) { PIN4(p0[w]); PIN4(p1[w]); }
    if (cw == 0) STAMP(5);
    const int vb0 = va0 ^ (cw * 64), vb1 = vb0 ^ 32;
#pragma unroll
    for (int w = 0; w < 4; ++w) {
      LAS char* S = L + ((NZ + 1 + w) % NS) * SLOT;
      lds_wait<0>(L_FULL, FE - 24 + 8 * w);
      if (w == 3 && cw == 1) STAMP(1);
      const v2u u0 = trread(S + vb0), u1 = trread(S + vb0 + 4096), u2 = trread(S + vb1), u3 = trread(S + vb1 + 4096);
      acc = mfma(v4u{u0[0], u0[1], u1[0], u1[1]}, p0[w], acc);
      acc = mfma(v4u{u2[0], u2[1], u3[0], u3[1]}, p1[w], acc);
      if (w < 3) PIN4(acc);
    }
    const bool rh = (i & 4) != 0;
    __builtin_amdgcn_raw_buffer_store_b128(v4u{__float_as_uint(rh ? acc[2] : acc[0]), tagA, __float_as_uint(rh ? acc[3] : acc[1]), tagB}, rw, W_O + bb * 4096 + cw * 1024 + lane * 16, 0, 16);
    if (cw == 0) STAMP(6);
  }
  {
    const int t = cw * 64 + lane;
    int wa4[4];
#pragma unroll
    for (int u = 0; u < 4; ++u) wa4[u] = (t * 4) ^ (swz(4 * u + cw) << 4);
    lds_wait<0>(L_FULL, FE + 24);
    lds_wait<0>(L_HRDY, 1);
    f32x2 wf[16], ya = {0.f, 0.f}, yb = {0.f, 0.f};
#pragma unroll
    for (int u = 0; u < 16; ++u) { const unsigned w = *(LAS unsigned*)(L + SL_WO * SLOT + u * 1024 + wa4[u & 3]); wf[u] = f32x2{lo16(w), hi16(w)}; }
    if (r32 < 28) {
      const f32x4 h0 = *(LAS f32x4*)(L + L_OV + 64), h1 = *(LAS f32x4*)(L + L_OV + 80);
#pragma unroll
      for (int u = 0; u < 4; ++u) {
        const unsigned wa = *(LAS unsigned*)(L + SL_W2 * SLOT + u * 1024 + wa4[u]), wb = *(LAS unsigned*)(L + SL_W2 * SLOT + (4 + u) * 1024 + wa4[u]);
        ya += f32x2{lo16(wa), hi16(wa)} * h0[u]; yb += f32x2{lo16(wb), hi16(wb)} * h1[u];
      }
    }
    int pa = W_P + b * 4096 + ((t * 16) ^ ((b & 31) << 7));
#pragma unroll
    for (int u = 0; u < 16; ++u) PIN4(wf[u]);
    PIN4(ya); PIN4(yb); PIN4(pa);
    bar();
    f32x4 c[4];
#pragma unroll
    for (int u = 0; u < 4; ++u) c[u] = *(LAS f32x4*)(L + L_OV + u * 16);
    asm volatile("" ::: "memory");
#pragma unroll
    for (int u = 0; u < 4; ++u) { ya += wf[4 * u] * c[u][0]; yb += wf[4 * u + 1] * c[u][1]; ya += wf[4 * u + 2] * c[u][2]; yb += wf[4 * u + 3] * c[u][3]; }
    ya += yb;
    __builtin_amdgcn_raw_buffer_store_b128(v4u{__float_as_uint(ya[0]), tagA, __float_as_uint(ya[1]), tagB}, rw, pa, 0, 16);
    if (cw == 0) STAMP(8);
    FLUSH();
  }
}

extern "C" void kernel_launch(void* const* d_in, const int* in_sizes, int n_in, void* d_out, int out_size, void* d_ws, size_t ws_size, hipStream_t stream) {
  (void)hipFuncSetAttribute((const void*)fused_decode, hipFuncAttributeMaxDynamicSharedMemorySize, L_END);
  fused_decode<<<NWG, NTHR, L_END, stream>>>((const u16*)d_in[0], (const u16*)d_in[1], (const u16*)d_in[2], (const u16*)d_in[3], (const u16*)d_in[4],
                                             (const u16*)d_in[6], (const u16*)d_in[7], (u16*)d_out, (char*)d_ws);
}
```

```cpp
#define CODE_KB 17
#include <hip/hip_runtime.h>
#include <stdint.h>

#define DBG 0
#define WA_ 27
#define WB_ 23
#define WC_ 21
#define WD_ 19
#define WE_ 17
#define BAND 30
#define ADAPT 0
#define TAU16 864
#define CK0 7
#define CKS 14
#define CKD 4
#define OWND 25
#define M17 0x11
#define M18 0x19
#define QDELAY 150
#define C1 105
#define C2 82
#define D2 25
#define ZR 58
#define RCAP 36
#define RO -65
#define TAUR 864
#define KD 20
#define WD 0
#define LEV0 0
#define PT 1
#define ZH 99

#define LAS __attribute__((address_space(3)))
typedef unsigned v4u __attribute__((ext_vector_type(4)));
typedef unsigned v2u __attribute__((ext_vector_type(2)));
typedef float f32x4 __attribute__((ext_vector_type(4)));
typedef float f32x2 __attribute__((ext_vector_type(2)));
typedef __bf16 bf16x8 __attribute__((ext_vector_type(8)));
typedef __bf16 bf16x2 __attribute__((ext_vector_type(2)));
typedef short s16x4 __attribute__((ext_vector_type(4)));
typedef unsigned short u16;

constexpr int D = 512, HQ = 32, HKV = 8, SK = 65536, DH = 128, DFF = 1792;
constexpr int NWG = 256, NTHR = 384;
constexpr int NF = 64, NS = 8, SLOT = 16384;
constexpr int L_SC = NS * SLOT,
              L_STG = L_SC + 16384,
              L_XN = L_STG + 8192, L_QB = L_XN + 1024, L_XR = L_QB + 1024, L_EB = L_XR + 1024, L_JUNK = L_EB + 1024, L_OV = L_JUNK + 1024,
              L_FULL = L_OV + 192, L_REL = L_FULL + 256, L_LEV = L_REL + 16, L_QRDY = L_REL + 20, L_HRDY = L_REL + 24, L_SY1 = L_REL + 28, L_SY2 = L_REL + 32,
              L_FRDY = L_REL + 36, L_RT = L_REL + 40, L_MX = L_REL + 128, L_LS = L_MX + 64, L_FAC = L_LS + 64, L_END = L_FAC + 16;
static_assert(L_END <= 163840, "LDS");
constexpr int W_E = 0, W_ML = 4096, W_T = 40960, W_Q = 65536, W_H = W_Q + 131072, W_O = W_H + 131072, W_P = W_O + (1 << 19), W_END = W_P + (1 << 19), W_DBG = 4 << 20;
constexpr unsigned SPIN_CAP = 1u << 16;
constexpr float LOG2E = 1.4426950408889634f, SCALE = 0.08838834764831845f;
constexpr unsigned FE = 64 + 16 * (NF + 1);

__device__ __forceinline__ unsigned cvtpk(float lo, float hi) { return __builtin_bit_cast(unsigned, __builtin_convertvector(f32x2{lo, hi}, bf16x2)); }
__device__ __forceinline__ float lo16(unsigned u) { return __uint_as_float(u << 16); }
__device__ __forceinline__ float hi16(unsigned u) { return __uint_as_float(u & 0xffff0000u); }
__device__ __forceinline__ float rbf(float x) { return lo16(cvtpk(x, x)); }
template <int CTRL> __device__ __forceinline__ float dpp(float x) { return __builtin_bit_cast(float, __builtin_amdgcn_mov_dpp(__builtin_bit_cast(int, x), CTRL, 0xf, 0xf, true)); }
constexpr int XOR1 = 0xB1, XOR2 = 0x4E, XOR7 = 0x141, ROR8 = 0x128, ROR4 = 0x124;
__device__ __forceinline__ float x16_32_max(float x) {
  auto s = __builtin_amdgcn_permlane16_swap(__float_as_uint(x), __float_as_uint(x), false, false);
  x = fmaxf(__uint_as_float(s[0]), __uint_as_float(s[1]));
  auto t = __builtin_amdgcn_permlane32_swap(__float_as_uint(x), __float_as_uint(x), false, false);
  return fmaxf(__uint_as_float(t[0]), __uint_as_float(t[1]));
}
__device__ __forceinline__ float x16_32_sum(float x) {
  auto s = __builtin_amdgcn_permlane16_swap(__float_as_uint(x), __float_as_uint(x), false, false);
  x = __uint_as_float(s[0]) + __uint_as_float(s[1]);
  auto t = __builtin_amdgcn_permlane32_swap(__float_as_uint(x), __float_as_uint(x), false, false);
  return __uint_as_float(t[0]) + __uint_as_float(t[1]);
}
__device__ __forceinline__ float x16_32_sum2(float a, float b) {
  auto s = __builtin_amdgcn_permlane16_swap(__float_as_uint(a), __float_as_uint(b), false, false);
  const float x = __uint_as_float(s[0]) + __uint_as_float(s[1]);
  auto t = __builtin_amdgcn_permlane32_swap(__float_as_uint(x), __float_as_uint(x), false, false);
  return __uint_as_float(t[0]) + __uint_as_float(t[1]);
}
__device__ __forceinline__ float row_sum(float x) { x += dpp<XOR1>(x); x += dpp<XOR2>(x); x += dpp<XOR7>(x); return x + dpp<ROR8>(x); }
__device__ __forceinline__ float wave_sum(float x) { return x16_32_sum(row_sum(x)); }
__device__ __forceinline__ float head_max(float x) { x = fmaxf(x, dpp<ROR8>(x)); x = fmaxf(x, dpp<ROR4>(x)); return x16_32_max(x); }
__device__ __forceinline__ float head_sum(float x) { x += dpp<ROR8>(x); x += dpp<ROR4>(x); return x16_32_sum(x); }
__device__ __forceinline__ f32x4 mfma(v4u a, v4u b, f32x4 c) { return __builtin_amdgcn_mfma_f32_16x16x32_bf16(__builtin_bit_cast(bf16x8, a), __builtin_bit_cast(bf16x8, b), c, 0, 0, 0); }
__device__ __forceinline__ v2u trread(LAS char* p) { return __builtin_bit_cast(v2u, __builtin_amdgcn_ds_read_tr16_b64_v4i16((LAS s16x4*)p)); }
__device__ __forceinline__ void bar() { asm volatile("s_waitcnt lgkmcnt(0)\n\ts_barrier" ::: "memory"); }
template <int SL> __device__ __forceinline__ unsigned lds_wait(int off, unsigned need) {
  unsigned v, n;
  if (SL == 0)
    asm volatile("s_mov_b32 %1, %4\n.Lw%=:\n\tds_read_b32 %0, %2\n\ts_sub_u32 %1, %1, 1\n\ts_waitcnt lgkmcnt(0)\n\tv_cmp_le_u32 vcc, %3, %0\n\ts_cbranch_scc1 .Lx%=\n\ts_cbranch_vccz .Lw%=\n.Lx%=:"
                 : "=&v"(v), "=&s"(n) : "v"(off), "s"(need), "n"(SPIN_CAP) : "memory", "vcc", "scc");
  else
    asm volatile("s_mov_b32 %1, %4\n.Lw%=:\n\tds_read_b32 %0, %2\n\ts_waitcnt lgkmcnt(0)\n\tv_cmp_le_u32 vcc, %3, %0\n\ts_cbranch_vccnz .Lx%=\n\ts_sleep %5\n\ts_sub_u32 %1, %1, 1\n\ts_cbranch_scc0 .Lw%=\n.Lx%=:"
                 : "=&v"(v), "=&s"(n) : "v"(off), "s"(need), "n"(SPIN_CAP), "n"(SL) : "memory", "vcc", "scc");
  return v;
}
__device__ __forceinline__ void lds_post(LAS char* L, int off, unsigned v, int lane) {
  asm volatile("s_waitcnt lgkmcnt(0)" ::: "memory");
  if (lane == 0) *(volatile LAS unsigned*)(L + off) = v;
}
__device__ __forceinline__ unsigned now32() { return (unsigned)__builtin_amdgcn_s_memrealtime(); }
__device__ __forceinline__ void wait_until(unsigned t) {
  unsigned n;
  t = __builtin_amdgcn_readfirstlane(t);
  asm volatile("s_mov_b32 %0, %2\n.Lw%=:\n\ts_memrealtime vcc\n\ts_sub_u32 %0, %0, 1\n\ts_cbranch_scc1 .Lx%=\n\ts_waitcnt lgkmcnt(0)\n\ts_sub_u32 vcc_lo, vcc_lo, %1\n\ts_cmp_lt_i32 vcc_lo, 0\n\ts_cbranch_scc1 .Lw%=\n.Lx%=:\n\ts_waitcnt lgkmcnt(0)"
               : "=&s"(n) : "s"(t), "n"(SPIN_CAP) : "memory", "vcc", "scc");
}
__device__ __forceinline__ void sync4(LAS char* L, int off, int lane) {
  asm volatile("s_waitcnt lgkmcnt(0)" ::: "memory");
  if (lane == 0) __hip_atomic_fetch_add((LAS unsigned*)(L + off), 1u, __ATOMIC_RELAXED, __HIP_MEMORY_SCOPE_WORKGROUP);
  lds_wait<1>(off, 4u);
}
#define STAMP(k) do {} while (0)
#define FLUSH() do {} while (0)
#define PROX 0
#define PIN(a) asm volatile("" : "+v"(a))

#define REC(a, c) v4u{__float_as_uint(a), tagA, __float_as_uint(c), tagB}
#define PUT(v, off) __builtin_amdgcn_raw_buffer_store_b128(v, rw, off, 0, 16)
#define GET(off) __builtin_amdgcn_raw_buffer_load_b128(rw, off, 0, 16)
#define TAGS_ACC(v, acc) __builtin_amdgcn_bitop3_b32((v)[3], tagB, __builtin_amdgcn_bitop3_b32((v)[1], tagA, acc, 0xBE), 0xBE)
template <int N> __device__ __forceinline__ unsigned tags_bad(const v4u (&v)[N], unsigned tagA, unsigned tagB) {
  unsigned a = 0, c = 0;
#pragma unroll
  for (int k = 0; k < N; ++k) { if (k & 1) c = TAGS_ACC(v[k], c); else a = TAGS_ACC(v[k], a); }
  return a | c;
}
#define GATHER(N, v, adr, off) do { _Pragma("unroll") for (int k = 0; k < N; ++k) v[k] = GET((adr) + (off)); REGATHER(N, v, adr, off); } while (0)
#define REGATHER(N, v, adr, off) do { \
    if (__builtin_expect(__any(tags_bad<N>(v, tagA, tagB) != 0), 0)) { \
      _Pragma("unroll 1") for (unsigned n_ = 0; n_ < SPIN_CAP && tags_bad<N>(v, tagA, tagB) != 0; ++n_) { asm volatile("" ::: "memory"); \
        _Pragma("unroll") for (int k = 0; k < N; ++k) v[k] = GET((adr) + (off)); } \
      __builtin_amdgcn_s_waitcnt(0x0F70); } } while (0)
#define F0(v) __uint_as_float((v)[0])
#define F1(v) __uint_as_float((v)[2])

template <int NST>
__device__ __forceinline__ f32x2 slice_matvec(LAS char* W, LAS char* xn, int grp, int i) {
  const int base = (8 * grp + (i >> 2)) * 128 + ((((i & 3) >> 1) ^ (2 * (((i >> 3) & 1) | ((grp & 1) << 1)))) << 4) + (i & 1) * 8;
  f32x4 a[4];
#pragma unroll
  for (int ct = 0; ct < 4; ++ct) a[ct] = f32x4{0.f, 0.f, 0.f, 0.f};
#pragma unroll
  for (int s = 0; s < NST; ++s) {
    const v4u xb = *(LAS v4u*)(xn + s * 64 + grp * 16);
#pragma unroll
    for (int ct = 0; ct < 4; ++ct) {
      const v2u e0 = trread(W + (base ^ (ct * 32)) + s * 4096), e1 = trread(W + (base ^ (ct * 32)) + s * 4096 + 512);
      a[ct] = mfma(v4u{e0[0], e0[1], e1[0], e1[1]}, xb, a[ct]);
    }
  }
  const f32x4 lo = (i & 2) ? a[1] : a[0], hi = (i & 2) ? a[3] : a[2], v = (i & 4) ? hi : lo;
  return (i & 1) ? f32x2{v[2], v[3]} : f32x2{v[0], v[1]};
}
__device__ __forceinline__ int swz(int row) { return 2 * ((row & 7) ^ (4 * (row >> 3))) + (row >> 3); }
__device__ __forceinline__ int jx_of(int j) { return (j << 19) | ((__builtin_popcount(j & M18) & 1) << 18) | ((__builtin_popcount(j & M17) & 1) << 17); }

#define STR_(x) #x
#define STR(x) STR_(x)
#define GATE(W) "s_waitcnt vmcnt(" STR(W) "-2)\n\tds_write_b32 %[fa], %[ld]\n\tv_add_u32 %[ld], 2, %[ld]\n\t"
#define LD2A(so) "buffer_load_dwordx4 %[v0], " so " offen nt lds\n\tbuffer_load_dwordx4 %[v1], " so " offen offset:1024 nt lds\n\t"
#define LD2B(so) "buffer_load_dwordx4 %[v2], " so " offen offset:2048 nt lds\n\tbuffer_load_dwordx4 %[v3], " so " offen offset:3072 nt lds\n\t"
#define FILL(W) \
  "s_lshl4_add_u32 s53, s52, 66-" STR(W) "\n\ts_mov_b32 m0, s48\n\tv_mov_b32 %[ld], s53\n\t" \
  GATE(W) LD2A("s[40:43], s54") \
  "s_add_u32 s55, s54, s62\n\ts_add_u32 s61, s54, s63\n\t" \
  GATE(W) LD2B("s[40:43], s54") \
  "s_add_u32 m0, m0, s63\n\ts_bfe_u32 s53, s52, 0x20003\n\ts_and_b32 s54, s52, 7\n\t" \
  GATE(W) LD2A("s[40:43], s55") \
  "s_lshl4_add_u32 s54, s53, s54\n\ts_lshl_b32 s54, s54, 13\n\ts_xor_b32 s54, s54, %[jx]\n\t" \
  GATE(W) LD2B("s[40:43], s55") \
  "s_add_u32 m0, m0, s63\n\ts_add_u32 s55, s55, s63\n\ts_add_u32 s52, s52, 1\n\t" \
  GATE(W) LD2A("s[40:43], s61") \
  "s_and_b32 s48, s52, 7\n\ts_lshl_b32 s48, s48, 14\n\ts_sub_u32 s60, s52, 8\n\t" \
  GATE(W) LD2B("s[40:43], s61") \
  "ds_read_b32 %[fr], %[fra]\n\ts_add_u32 m0, m0, s63\n\t" \
  GATE(W) LD2A("s[40:43], s55") \
  "s_waitcnt lgkmcnt(1)\n\tv_readlane_b32 s53, %[fr], 4\n\tv_cmp_gt_i32 vcc, s60, %[fr]\n\t" \
  GATE(W) LD2B("s[40:43], s55") \
  "s_and_b32 s53, s53, 7\n\ts_min_u32 s53, s53, 4\n\ts_mul_i32 s53, s53, s64\n\ts_cmp_eq_u32 s52, " STR(ZR) "\n\ts_cselect_b32 s53, s66, s53\n\t" \
  "s_add_u32 s56, s58, s53\n\ts_addc_u32 s57, s59, 0\n\ts_cmp_eq_u32 s52, 33\n\ts_cselect_b64 s[40:41], %[vb], s[40:41]\n\t" \
  "s_cbranch_vccnz .Lstall%=\n\ts_setpc_b64 s[56:57]\n"
#define DRAIN(n) "s_waitcnt vmcnt(" #n ")\n\tds_write_b32 %[fa], %[ld]\n\tv_add_u32 %[ld], 8, %[ld]\n\t"

__global__ void __launch_bounds__(NTHR) fused_decode(const u16* __restrict__ x, const u16* __restrict__ Kc, const u16* __restrict__ Vc, const u16* __restrict__ wq,
                                                     const u16* __restrict__ wo, const u16* __restrict__ w1, const u16* __restrict__ w2, u16* __restrict__ out, char* ws) {
  extern __shared__ __attribute__((aligned(16))) char lds_raw[];
  int vo[4], vt0, vt1;
#define LD4(m, so) "s_mov_b32 m0, " m "\n\t" so LD2A("s[44:47], s42") LD2B("s[44:47], s42")
  asm volatile(
      "s_load_dwordx16 s[48:63], %[kp], 0x0\n\ts_load_dwordx2 s[64:65], %[kp], 0x40\n\t"
      "v_readfirstlane_b32 s40, %[tid]\n\tv_and_b32 %[t0], 63, %[tid]\n\ts_lshr_b32 s41, %[b], 3\n\ts_mov_b32 s47, 0x20000\n\tv_lshlrev_b32 %[t1], 4, %[t0]\n\t"
      "s_cmp_lg_u32 s40, 0\n\ts_cbranch_scc1 .Lnl%=\n\t"
      "s_getpc_b64 s[44:45]\n\ts_lshl_b32 s42, s41, 10\n\ts_and_b32 s44, s44, 0xffffff00\n\ts_and_b32 s45, s45, 0xffff\n\ts_movk_i32 s46, %[code]\n\t"
      "s_mov_b32 m0, %[junk]\n\tv_add_u32 %[v1], s42, %[t1]\n\ts_setprio 3\n\tbuffer_load_dwordx4 %[v1], s[44:47], 0 offen lds\n\t"
      "v_lshlrev_b32 %[t0], 1, %[t0]\n\ts_and_b32 s42, %[b], 7\n\tv_and_b32 %[t0], 0x60, %[t0]\n\ts_lshl_b32 s42, s42, 5\n\tv_xor_b32 %[v0], %[t0], %[t1]\n\ts_add_u32 s42, s42, s41\n\t"
      "v_xor_b32 %[v1], 0x80, %[v0]\n\ts_and_b32 s43, s41, %[m18]\n\tv_xor_b32 %[v2], 0x90, %[v0]\n\ts_bcnt1_i32_b32 s43, s43\n\tv_xor_b32 %[v3], 16, %[v0]\n\ts_and_b32 s43, s43, 1\n\t"
      "s_lshl1_add_u32 s42, s42, s43\n\ts_and_b32 s43, s41, %[m17]\n\ts_bcnt1_i32_b32 s43, s43\n\ts_and_b32 s43, s43, 1\n\ts_lshl1_add_u32 s42, s42, s43\n\ts_lshl_b32 s42, s42, 17\n\t"
      "s_brev_b32 s46, 16\n\ts_waitcnt lgkmcnt(0)\n\ts_mov_b32 s44, s50\n\t"
      "v_mov_b32 %[t0], s42\n\ts_lshr_b32 s43, s42, 17\n\ts_and_b32 s43, s43, 0x380\n\tglobal_load_lds_dword %[t0], s[50:51] nt\n\tv_mov_b32 %[t1], s43\n\tglobal_load_lds_dword %[t0], s[52:53] nt\n\t"
      "global_load_lds_dword %[t1], s[62:63]\n\t"
      "s_barrier\n\ts_sleep " STR(KD) "\n\t"
      LD4("0x4000", "s_and_b32 s45, s51, 0xffff\n\t") LD4("0x5000", "s_add_u32 s42, s42, 0x10000\n\t") LD4("0x6000", "s_sub_u32 s42, s42, 0xf000\n\t") LD4("0x7000", "s_add_u32 s42, s42, 0x10000\n\t")
      "s_branch .Lend2%=\n"
      ".Lnl%=:\n\ts_cmpk_lg_u32 s40, 320\n\ts_cbranch_scc1 .Lcw%=\n\t"
      "s_setprio 3\n\tv_lshlrev_b32 %[v0], 1, %[t0]\n\ts_and_b32 s42, s41, 3\n\tv_and_b32 %[v0], 0x20, %[v0]\n\ts_lshl_b32 s42, s42, 10\n\tv_and_b32 %[v1], 0x70, %[t1]\n\ts_and_b32 s43, %[b], 7\n\t"
      "v_xor_b32 %[v0], %[v0], %[v1]\n\ts_add_u32 s42, s42, s43\n\tv_lshlrev_b32 %[v1], 10, %[t0]\n\ts_lshr_b32 s43, s41, 2\n\tv_and_b32 %[v1], 0xe000, %[v1]\n\ts_lshl3_add_u32 s42, s42, s43\n\t"
      "v_or_b32 %[v0], %[v0], %[v1]\n\ts_lshl_b32 s42, s42, 7\n\tv_xor_b32 %[v1], 64, %[v0]\n\tv_add_u32 %[v2], 0x1f800, %[v0]\n\tv_add_u32 %[v1], 0xfc00, %[v1]\n\ts_mov_b32 s46, 0x400000\n\t"
      "v_add_u32 %[v3], 0x1f800, %[v1]\n\tv_mov_b32 %[t0], 0\n\ts_mov_b32 m0, %[xr]\n\ts_waitcnt lgkmcnt(0)\n\tglobal_load_lds_dwordx4 %[t1], s[48:49]\n\t"
      "s_mov_b32 m0, %[eb]\n\ts_mov_b32 s44, s54\n\tglobal_load_lds_dwordx4 %[t0], s[64:65] sc1\n\t"
      LD4("%[sc]", "s_and_b32 s45, s55, 0xffff\n\t") LD4("%[sc]+0x1000", "s_add_u32 s42, s42, 0x40000\n\t") LD4("%[sc]+0x2000", "s_add_u32 s42, s42, 0x40000\n\t") LD4("%[sc]+0x3000", "s_add_u32 s42, s42, 0x40000\n\t")
      "v_lshrrev_b32 %[t0], 13, %[v0]\n\tv_and_b32 %[v0], 0x70, %[v0]\n\ts_and_b32 s43, %[b], 7\n\tv_mul_u32_u24 %[t0], 0xe00, %[t0]\n\ts_mul_i32 s43, s43, 28\n\ts_add_u32 s43, s43, s41\n\t"
      "v_or_b32 %[v0], %[v0], %[t0]\n\ts_and_b32 s42, s43, 7\n\tv_xor_b32 %[v1], 64, %[v0]\n\ts_mul_i32 s42, s42, 0x38000\n\tv_add_u32 %[v2], 0xd800, %[v0]\n\ts_lshr_b32 s43, s43, 3\n\t"
      "v_add_u32 %[v1], 0x6c00, %[v1]\n\ts_lshl_b32 s43, s43, 7\n\ts_add_u32 s42, s42, s43\n\tv_add_u32 %[v3], 0xd800, %[v1]\n\ts_cmp_lt_u32 s41, 28\n\ts_cselect_b32 s46, 0x1c0000, 0\n\ts_mov_b32 s44, s58\n\t"
      LD4("%[stg]", "s_and_b32 s45, s59, 0xffff\n\t") LD4("%[stg]+0x1000", "s_add_u32 s42, s42, 0x1c000\n\t")
      "s_branch .Lend%=\n"
      ".Lcw%=:\n\tv_and_b32 %[v2], 0x70, %[t1]\n\ts_sub_u32 s40, s40, 64\n\tv_lshlrev_b32 %[v0], 13, %[t0]\n\ts_and_b32 s42, s41, 1\n\tv_lshlrev_b32 %[v1], 11, %[t0]\n\ts_lshl_b32 s42, s42, 8\n\t"
      "v_and_b32 %[v0], 0x60000, %[v0]\n\ts_add_u32 s42, s42, s40\n\tv_and_b32 %[v1], 0x4000, %[v1]\n\ts_lshl_b32 s42, s42, 1\n\tv_or3_b32 %[v0], %[v0], %[v1], %[v2]\n\t"
      "s_and_b32 s43, %[b], 7\n\tv_lshlrev_b32 %[v1], 8, %[t0]\n\ts_mul_i32 s66, s43, 14\n\tv_and_b32 %[v1], 0x3800, %[v1]\n\ts_lshr_b32 s67, s41, 1\n\tv_or_b32 %[v1], %[v1], %[v2]\n\t"
      "s_add_u32 s66, s66, s67\n\ts_lshl_b32 s66, s66, 14\n\ts_add_u32 s66, s66, s42\n\t"
      "s_lshl_b32 s43, s43, 4\n\ts_lshr_b32 s46, s41, 3\n\ts_add_u32 s43, s43, s46\n\ts_and_b32 s67, s67, 3\n\ts_lshl3_add_u32 s43, s43, s67\n\ts_lshl_b32 s43, s43, 12\n\ts_add_u32 s42, s42, s43\n\t"
      "s_mul_i32 s40, s40, 96\n\ts_add_u32 m0, s40, 0x18000\n\ts_mov_b32 s46, 0x400000\n\ts_cmp_lt_u32 s41, 28\n\ts_cselect_b32 s67, 0x1c0000, 0\n\t"
      "v_lshlrev_b32 %[t1], 2, %[t0]\n\tv_mov_b32 %[v2], 0\n\tv_mov_b32 %[v3], 0\n\tv_add_u32 %[t1], %[full], %[t1]\n\ts_movk_i32 s43, %[lev]\n\t"
      "v_writelane_b32 %[v3], -3, 32\n\tv_writelane_b32 %[v3], -3, 33\n\tv_writelane_b32 %[v3], -3, 34\n\tv_writelane_b32 %[v3], -3, 35\n\tv_writelane_b32 %[v3], s43, 36\n\t"
      "s_waitcnt lgkmcnt(0)\n\ts_mov_b32 s44, s56\n\ts_and_b32 s45, s57, 0xffff\n\t"
      "buffer_load_dwordx4 %[v0], s[44:47], s42 offen nt lds\n\tbuffer_load_dwordx4 %[v0], s[44:47], s42 offen offset:1024 nt lds\n\t"
      "buffer_load_dwordx4 %[v0], s[44:47], s42 offen offset:2048 nt lds\n\tbuffer_load_dwordx4 %[v0], s[44:47], s42 offen offset:3072 nt lds\n\t"
      "s_add_u32 m0, m0, 0x1000\n\ts_and_b32 s61, s61, 0xffff\n\ts_mov_b32 s62, s67\n\ts_mov_b32 s63, s47\n\t"
      "buffer_load_dwordx4 %[v1], s[60:63], s66 offen nt lds\n\tbuffer_load_dwordx4 %[v1], s[60:63], s66 offen offset:1024 nt lds\n\t"
      "ds_write_b32 %[t1], %[v2]\n\tds_write_b32 %[t1], %[v3] offset:128\n"
      ".Lend%=:\n\ts_waitcnt lgkmcnt(0)\n\ts_barrier\n.Lend2%=:"
      : [v0] "=&v"(vo[0]), [v1] "=&v"(vo[1]), [v2] "=&v"(vo[2]), [v3] "=&v"(vo[3]), [t0] "=&v"(vt0), [t1] "=&v"(vt1)
      : [kp] "s"(__builtin_amdgcn_kernarg_segment_ptr()), [b] "s"(blockIdx.x), [tid] "v"(threadIdx.x),
        [code] "n"(CODE_KB * 1024), [junk] "n"(L_JUNK), [m17] "n"(M17), [m18] "n"(M18), [xr] "n"(L_XR), [eb] "n"(L_EB), [sc] "n"(L_SC), [stg] "n"(L_STG), [full] "n"(L_FULL), [lev] "n"(0x400 + LEV0)
      : "memory", "scc", "s40", "s41", "s42", "s43", "s44", "s45", "s46", "s47", "s48", "s49", "s50", "s51", "s52", "s53", "s54", "s55", "s56", "s57", "s58", "s59", "s60", "s61", "s62", "s63", "s64", "s65", "s66", "s67");
  LAS char* L = (LAS char*)lds_raw;
  const int b = blockIdx.x, lane = threadIdx.x & 63, wave = __builtin_amdgcn_readfirstlane(threadIdx.x >> 6);
  const int g = b & 7, j = b >> 3;
  const int bb = g * 32 + j;
  const int grp = lane >> 4, i = lane & 15;
  const bool svc = wave == 5;
  const int ff = g * 28 + (j < 28 ? j : 27);

  if (wave == 0) {
    constexpr unsigned long long A48 = 0xffffffffffffull;
    int ld, fr;
    static_assert(RCAP <= 48 && ZR > 33 && ZR <= 64 && NS == 8 && NF == 64, "loader constants");
    asm volatile(
        "s_mov_b64 s[40:41], %[kb]\n\ts_mov_b32 s42, 0x1000000\n\ts_mov_b32 s43, 0x20000\n\ts_mov_b32 s48, 0x8000\n\ts_mov_b32 s52, 2\n\ts_xor_b32 s54, %[jx], 0x2000\n\t"
        "s_mov_b32 s62, 0x10000\n\ts_movk_i32 s63, 0x1000\n\ts_mov_b32 s64, .Lv1%=-.Lv0%=\n\ts_mov_b32 s66, .Lexit%=-.Lv0%=\n\t"
        "s_getpc_b64 s[58:59]\n"
        ".Lv0%=:\n\t" FILL(WC_) ".Lv1%=:\n\t" FILL(WA_) FILL(WB_) FILL(WD_) FILL(WE_)
        ".Lstall%=:\n\ts_mov_b32 s53, %[cap]\n"
        ".Lst%=:\n\tds_read_b32 %[fr], %[fra]\n\ts_sub_u32 s53, s53, 1\n\ts_waitcnt lgkmcnt(0)\n\tv_cmp_gt_i32 vcc, s60, %[fr]\n\ts_cbranch_scc1 .Lsx%=\n\ts_cbranch_vccnz .Lst%=\n"
        ".Lsx%=:\n\ts_setpc_b64 s[56:57]\n"
        ".Lexit%=:\n\tds_read_b32 %[fr], %[fa] offset:%[rt]\n\ts_memrealtime s[56:57]\n\t"
        "s_movk_i32 s52, 4*(" STR(ZR) "-33)\n\tv_mov_b32 %[ld], 64+16*" STR(ZR) "+4-" STR(RCAP) "\n\ts_mov_b32 s65, %[cap]\n\ts_waitcnt lgkmcnt(0)\n\tv_readfirstlane_b32 s53, %[fr]\n\t"
        "s_nop 3\n\ts_sub_u32 s54, s53, s56\n\ts_abs_i32 s54, s54\n\ts_cmpk_lt_u32 s54, 400\n\ts_cselect_b32 s53, s53, s56\n\ts_lshl_b32 s53, s53, 6\n"
        ".Lr%=:\n\ts_lshr_b32 s54, s52, 5\n\ts_and_b32 s55, s52, 1\n\ts_lshl1_add_u32 s54, s54, s55\n\ts_bfe_u32 s55, s52, 0x40001\n\ts_lshl4_add_u32 s54, s54, s55\n\ts_lshl_b32 s54, s54, 12\n\ts_xor_b32 s54, s54, %[jx]\n\t"
        "s_add_u32 s55, s52, 4\n\ts_and_b32 s55, s55, 31\n\ts_lshl_b32 m0, s55, 12\n\ts_lshr_b32 s60, s52, 2\n\ts_add_u32 s60, s60, 25\n"
        ".Ls%=:\n\ts_memrealtime s[56:57]\n\tds_read_b32 %[fr], %[fra]\n\ts_sub_u32 s65, s65, 1\n\ts_cbranch_scc1 .Lg%=\n\ts_waitcnt lgkmcnt(0)\n\tv_cmp_gt_i32 vcc, s60, %[fr]\n\t"
        "s_lshl_b32 s56, s56, 6\n\ts_sub_u32 s56, s56, s53\n\ts_cmp_lt_i32 s56, 0\n\ts_cbranch_scc1 .Ls%=\n\ts_cbranch_vccnz .Ls%=\n"
        ".Lg%=:\n\ts_waitcnt vmcnt(" STR(RCAP) "-4) lgkmcnt(0)\n\t" LD2A("s[40:43], s54") LD2B("s[40:43], s54")
        "ds_write_b32 %[fa], %[ld]\n\tv_add_u32 %[ld], 4, %[ld]\n\ts_max_i32 s65, s65, 0\n\ts_add_u32 s53, s53, " STR(TAUR) "\n\ts_add_u32 s52, s52, 1\n\ts_cmpk_lg_u32 s52, 128\n\ts_cbranch_scc1 .Lr%=\n\t"
        "v_mov_b32 %[ld], %[fin]-40\n\t" DRAIN(40) DRAIN(32) DRAIN(24) DRAIN(16) DRAIN(8) DRAIN(0) "s_waitcnt lgkmcnt(0)"
        : [ld] "=&v"(ld), [fr] "=&v"(fr)
        : [v0] "v"(vo[0]), [v1] "v"(vo[1]), [v2] "v"(vo[2]), [v3] "v"(vo[3]), [fa] "v"(L_FULL + lane * 4), [fra] "v"(L_REL + (lane <= 4 ? lane : 0) * 4),
          [cap] "n"(SPIN_CAP), [fin] "n"(FE), [rt] "n"(L_RT - L_FULL), [jx] "s"(jx_of(j)),
          [kb] "s"(((unsigned long long)Kc + (g << 24)) & A48), [vb] "s"(((unsigned long long)Vc + (g << 24)) & A48)
        : "memory", "vcc", "scc", "s40", "s41", "s42", "s43", "s44", "s45", "s46", "s47", "s48", "s49", "s50", "s51", "s52", "s53", "s54", "s55", "s56", "s57", "s58", "s59",
          "s60", "s61", "s62", "s63", "s64", "s65", "s66");
    return;
  }
  auto rw = __builtin_amdgcn_make_buffer_rsrc((void*)ws, 0, W_END, 0x00020000);

  if (svc) {
    STAMP(0);
    const int pc = (bb & ~31) + 4 * (bb & 7) + ((bb >> 4) & 1) + 2 * ((bb >> 3) & 1);
    v4u xv; v2u E; unsigned xr;
    asm volatile("s_waitcnt vmcnt(24)\n\tds_read_b128 %0, %3\n\tds_read_b64 %1, %4\n\tds_read_b32 %2, %5\n\ts_waitcnt lgkmcnt(0)"
                 : "=&v"(xv), "=&v"(E), "=&v"(xr) : "v"(L_XR + lane * 16), "v"(L_EB + W_E), "v"(L_XR + pc * 4) : "memory");
    const unsigned tagA = E[0] + 1u, tagB = E[1] + 1u;
    v4u xn;
    {
      float xf[8];
#pragma unroll
      for (int k = 0; k < 4; ++k) { xf[2 * k] = lo16(xv[k]); xf[2 * k + 1] = hi16(xv[k]); }
      const float mean = wave_sum(((xf[0] + xf[1]) + (xf[2] + xf[3])) + ((xf[4] + xf[5]) + (xf[6] + xf[7]))) * (1.f / D);
      float v = 0.f;
#pragma unroll
      for (int k = 0; k < 8; ++k) { const float d = xf[k] - mean; v += d * d; }
      const float var = wave_sum(v) * (1.f / D);
      const float mb = rbf(mean), rd = 1.f / rbf(__builtin_amdgcn_sqrtf(rbf(rbf(var) + 1e-5f)));
#pragma unroll
      for (int k = 0; k < 4; ++k) { const unsigned d = cvtpk(xf[2 * k] - mb, xf[2 * k + 1] - mb); xn[k] = cvtpk(lo16(d) * rd, hi16(d) * rd); }
    }
    PIN(xn);
    asm volatile("s_waitcnt vmcnt(8)" ::: "memory");
    *(LAS v4u*)(L + L_XN + lane * 16) = xn;
    STAMP(2);
    const f32x2 qp = slice_matvec<4>(L + L_SC, L + L_XN + (j & 3) * 256, grp, i);
    if (i < 8) PUT(REC(qp[0], qp[1]), W_Q + bb * 512 + (grp * 8 + i) * 16);
    asm volatile("" ::: "memory");
    const unsigned tq = now32();
    STAMP(3);
    asm volatile("s_waitcnt vmcnt(1)" ::: "memory");
    const f32x2 hp = slice_matvec<2>(L + L_STG, L + L_XN + (ff & 7) * 128, grp, i);
    if (i < 8 && j < 28) PUT(REC(hp[0], hp[1]), W_H + ff * 512 + (grp * 8 + i) * 16);
    wait_until(tq + QDELAY);
    {
      v4u qq[16];
      GATHER(16, qq, W_Q + (g * 32 + 4 * (lane >> 5)) * 512 + (lane & 31) * 16, (k >> 2) * 4096 + (k & 3) * 512);
      const int pos = lane & 31, pi = 8 * ((pos >> 1) & 3) + 2 * (pos >> 3) + (pos & 1);
#pragma unroll
      for (int h = 0; h < 4; ++h)
        *(LAS unsigned*)(L + L_QB + h * 256 + (lane >> 5) * 128 + pi * 4) =
            cvtpk((F0(qq[4 * h]) + F0(qq[4 * h + 1])) + (F0(qq[4 * h + 2]) + F0(qq[4 * h + 3])), (F1(qq[4 * h]) + F1(qq[4 * h + 1])) + (F1(qq[4 * h + 2]) + F1(qq[4 * h + 3])));
      lds_post(L, L_QRDY, 1u, lane);
    }
    STAMP(4);
    __builtin_amdgcn_s_setprio(0);
    unsigned tc = 0, sb = 0, tr = 0;
    int ck = 0, c = CK0, cp = 0, tau = TAU16; bool have = false;
    float mo = 0.f;
    const int hpi = 8 * ((ff >> 1) & 3) + (lane & 7);
    int ha = W_H + ((ff & ~7) + (lane >> 3)) * 512 + (8 * ((hpi >> 1) & 3) + 2 * (hpi >> 3) + (hpi & 1)) * 16;
#define HFIN(hv) do { REGATHER(1, hv, ha, 0); float h = x16_32_sum2(F0(hv[0]), F1(hv[0])); h = fmaxf(rbf(h + dpp<ROR8>(h)), 0.f); \
      *(LAS float*)(L + L_OV + 128 + (lane & 7) * 8 + (grp & 1) * 4) = h; lds_post(L, L_HRDY, 1u, lane); } while (0)
#pragma unroll 1
    for (int z = CK0; z <= ZR - 2; ++z) {
      const unsigned th = 64 + 16 * z;
      const unsigned fv = __builtin_amdgcn_readfirstlane(lds_wait<1>(L_FULL, th));
      const unsigned t = now32() - (((fv - th) * TAU16) >> 8);
      if (have) {
        const int lag = (int)(t - sb) - (((z - cp) * tau) >> 4);
        const unsigned lev = lag >= 3 * BAND / 2 ? 1 : lag >= BAND / 2 ? 2 : lag >= -BAND / 2 ? 0 : lag >= -3 * BAND / 2 ? 3 : 4;
        if (lane == 0) *(volatile LAS unsigned*)(L + L_LEV) = 0x400 + lev;
      }
      if (z == c) {
        tc = t;
        if ((j & 15) == 5 && lane < 8) PUT((v4u{tc, tagA, tc, tagB}), W_T + ck * 2048 + (g * 2 + (j >> 4)) * 128 + lane * 16);
      }
      if (z == c + CKD) {
        const v4u ra = GET(lane >= 16 ? W_END : W_T + ck * 2048 + i * 128);
        const bool ok = TAGS_ACC(ra, 0u) == 0;
        const float sum = row_sum(ok ? __builtin_amdgcn_fmed3f((float)(int)(ra[0] - tc), -400.f, 400.f) : 0.f), cnt = row_sum(ok ? 1.f : 0.f);
        const unsigned sn = tc + __builtin_amdgcn_readfirstlane((int)(sum * __builtin_amdgcn_rcpf(fmaxf(cnt, 1.f))));
        if (ADAPT && have) { const int tn = (int)(sn - sb) * 16 / CKS; tau = tn < TAU16 - 20 ? TAU16 - 20 : tn > TAU16 + ADAPT ? TAU16 + ADAPT : tn; }
        sb = sn; cp = c;
        have = true; ++ck; c += CKS;
        tr = sb + (((ZR - cp) * tau) >> 4) + RO;
        if (lane == 0) *(volatile LAS unsigned*)(L + L_RT) = tr;
      }
      if (z == ZH) { v4u hv[1]; hv[0] = GET(ha); HFIN(hv); }
      if (z == 37) {
        lds_wait<1>(L_SY2, 4u);
        const f32x4 mw = *(LAS f32x4*)(L + L_MX + (lane & 3) * 16), lw = *(LAS f32x4*)(L + L_LS + (lane & 3) * 16);
        mo = fmaxf(fmaxf(mw[0], mw[1]), fmaxf(mw[2], mw[3]));
        if (lane < 8) PUT(REC(mo, (lw[0] + lw[1]) + (lw[2] + lw[3])), W_ML + bb * 128 + lane * 16);
      }
      if (z == 44) {
        v4u gm[2];
        GATHER(2, gm, W_ML + (g * 32 + (lane >> 2)) * 128 + (lane & 3) * 16, k * 16 * 128);
        const float M = head_max(fmaxf(F0(gm[0]), F0(gm[1])));
        const float Ls = head_sum(__builtin_amdgcn_exp2f((F0(gm[0]) - M) * LOG2E) * F1(gm[0]) + __builtin_amdgcn_exp2f((F0(gm[1]) - M) * LOG2E) * F1(gm[1]));
        if (lane < 4) *(LAS float*)(L + L_FAC + lane * 4) = __builtin_amdgcn_exp2f((mo - M) * LOG2E) / Ls;
        lds_post(L, L_FRDY, 1u, lane);
      }
      if (z == 47 && b == 0) {
        v4u ga[4];
        GATHER(4, ga, W_ML + lane * 128, k * 64 * 128);
        if (lane == 0) __builtin_amdgcn_raw_buffer_store_b64(v2u{tagA, tagB}, rw, W_E, 0, 16);
      }
    }
    const unsigned t1 = tr + (((NF + 1 - ZR) * TAUR) >> 4) + C1;
    int ao = W_O + (g * 32 + (lane >> 3)) * 2048 + (j >> 1) * 128 + (lane & 7) * 16;
    int ova = L_OV + ((lane & 3) * 2 + ((lane >> 2) & 1)) * 16 + ((grp & 1) * 2 + (grp >> 1)) * 4;
    int ap = W_P + (2 * lane + (bb >> 7)) * 2048 + (((bb & 127) * 16) ^ ((lane & 15) << 7));
    PIN(ao); PIN(ova); PIN(ap);
    lds_wait<1>(L_FULL, FE);
    STAMP(15);
    v4u hv[1];
    if (ZH > NF) hv[0] = GET(ha);
    { const unsigned t2 = now32() + OWND; wait_until((int)(t2 - t1) > 0 ? t2 : t1); }
    STAMP(12);
    v4u gv[4];
#pragma unroll
    for (int k = 0; k < 4; ++k) gv[k] = GET(ao + k * 8 * 2048);
    if (ZH > NF) { asm volatile("" ::: "memory"); HFIN(hv); }
    REGATHER(4, gv, ao, k * 8 * 2048);
    STAMP(7);
    {
      const f32x2 oa = (f32x2{lo16(gv[0][0]), hi16(gv[0][0])} + f32x2{lo16(gv[1][0]), hi16(gv[1][0])}) + (f32x2{lo16(gv[2][0]), hi16(gv[2][0])} + f32x2{lo16(gv[3][0]), hi16(gv[3][0])});
      const f32x2 ob = (f32x2{lo16(gv[0][2]), hi16(gv[0][2])} + f32x2{lo16(gv[1][2]), hi16(gv[1][2])}) + (f32x2{lo16(gv[2][2]), hi16(gv[2][2])} + f32x2{lo16(gv[3][2]), hi16(gv[3][2])});
      auto s0 = __builtin_amdgcn_permlane16_swap(__float_as_uint(oa[0]), __float_as_uint(ob[0]), false, false);
      auto s1 = __builtin_amdgcn_permlane16_swap(__float_as_uint(oa[1]), __float_as_uint(ob[1]), false, false);
      auto s2 = __builtin_amdgcn_permlane32_swap(__float_as_uint(__uint_as_float(s0[0]) + __uint_as_float(s0[1])), __float_as_uint(__uint_as_float(s1[0]) + __uint_as_float(s1[1])), false, false);
      float ov = __uint_as_float(s2[0]) + __uint_as_float(s2[1]);
      ov += dpp<ROR8>(ov);
      *(LAS float*)(L + ova) = rbf(ov);
    }
    bar();
    { const unsigned t2 = now32() + D2; wait_until((int)(t2 - t1) > C2 ? t2 : t1 + C2); }
    v4u gp[2];
    GATHER(2, gp, ap, k * 128 * 2048);
    const float y = row_sum(x16_32_sum2(F0(gp[0]) + F0(gp[1]), F1(gp[0]) + F1(gp[1])));
    const float y1 = __uint_as_float(__builtin_amdgcn_readlane(__float_as_uint(y), 16));
    if (lane == 0) *(unsigned*)(out + 2 * pc) = cvtpk(lo16(xr) + y, hi16(xr) + y1);
    STAMP(9); FLUSH();
    return;
  }

  const int cw = wave - 1, c4 = i >> 2;
  v4u wv[6];
  asm volatile("s_waitcnt vmcnt(0)" ::: "memory");
#pragma unroll
  for (int k = 0; k < 6; ++k) wv[k] = *(LAS v4u*)(L + 6 * SLOT + cw * 6144 + k * 1024 + lane * 16);
  lds_post(L, L_REL + cw * 4, cw, lane);
  {
    const int ka0 = i * 256 + ((grp ^ swz(i)) << 4);
    const int sa0 = L_SC + (c4 >> 1) * 256 + (grp * 4 + (i & 3)) * 16 + (c4 & 1) * 8;
    v4u qm[4][4];
    float m = -INFINITY;
#pragma unroll 1
    for (int z = cw + 1; z <= NF / 2; z += 4) {
      LAS char* S = L + (z & (NS - 1)) * SLOT;
      v4u kf[4][4];
      lds_wait<2>(L_FULL, 64 + 16 * z + 16);
      if (z == 1) STAMP(14);
#pragma unroll
      for (int s = 0; s < 4; ++s)
#pragma unroll
        for (int u = 0; u < 4; ++u) kf[u][s] = *(LAS v4u*)(S + (ka0 ^ (s * 64)) + u * 4096);
      lds_post(L, L_REL + cw * 4, z + 3 < NF / 2 ? z + 3 : NF / 2, lane);
      if (z <= 4) {
        lds_wait<2>(L_QRDY, 1);
#pragma unroll
        for (int s = 0; s < 4; ++s) {
          const v4u qf = *(LAS v4u*)(L + L_QB + (i & 3) * 256 + s * 64 + grp * 16);
#pragma unroll
          for (int u = 0; u < 4; ++u) qm[u][s] = c4 == u ? qf : v4u{0u, 0u, 0u, 0u};
        }
      }
      f32x4 sc = {0.f, 0.f, 0.f, 0.f};
#pragma unroll
      for (int s = 0; s < 4; ++s)
#pragma unroll
        for (int u = 0; u < 4; ++u) sc = mfma(kf[u][s], qm[u][s], sc);
      const unsigned r0 = cvtpk(sc[0], sc[1]), r1 = cvtpk(sc[2], sc[3]);
      const unsigned x0 = cvtpk(lo16(r0) * SCALE, hi16(r0) * SCALE), x1 = cvtpk(lo16(r1) * SCALE, hi16(r1) * SCALE);
      *(LAS v2u*)(L + sa0 + (z - 1) * 512) = v2u{x0, x1};
      m = fmaxf(fmaxf(m, fmaxf(lo16(x0), hi16(x0))), fmaxf(lo16(x1), hi16(x1)));
    }
    m = head_max(m);
    if (lane < 4) *(LAS float*)(L + L_MX + lane * 16 + cw * 4) = m;
    sync4(L, L_SY1, lane);
    const f32x4 mw = *(LAS f32x4*)(L + L_MX + (lane & 3) * 16);
    const float Mn = -fmaxf(fmaxf(mw[0], mw[1]), fmaxf(mw[2], mw[3]));
    float l = 0.f;
#pragma unroll
    for (int k = 0; k < 4; ++k) {
      LAS v4u* p = (LAS v4u*)(L + L_SC + (cw + 8 * k + 4 * (lane >> 5)) * 512 + (lane & 31) * 16);
      v4u v = *p;
#pragma unroll
      for (int r = 0; r < 4; ++r) {
        const float e0 = __builtin_amdgcn_exp2f((lo16(v[r]) + Mn) * LOG2E), e1 = __builtin_amdgcn_exp2f((hi16(v[r]) + Mn) * LOG2E);
        l += e0 + e1; v[r] = cvtpk(e0, e1);
      }
      *p = v;
    }
    l = head_sum(l);
    if (lane < 4) *(LAS float*)(L + L_LS + lane * 16 + cw * 4) = l;
    sync4(L, L_SY2, lane);
  }
  if (cw == 0 && !PROX) STAMP(5);
  int pa, oc; f32x2 wf[16], y[4];
  const v2u E = *(LAS v2u*)(L + L_EB + W_E);
  unsigned tagA = E[0] + 1u, tagB = E[1] + 1u;
  {
    const int vb0 = ((4 * grp + c4) * 256 + ((((i & 3) >> 1) ^ swz(4 * grp + c4)) << 4) + (i & 1) * 8) ^ (cw * 64);
    const int pa0 = L_SC + (grp * 4 + (i & 3)) * 16;
    int oa = i < 8 ? W_O + bb * 2048 + cw * 512 + grp * 128 + i * 16 : W_END;
    f32x4 t0 = {0.f, 0.f, 0.f, 0.f}, t1 = {0.f, 0.f, 0.f, 0.f};
#define STAGE(t, rel) do { const int va = (((t) + 2) & (2 * NS - 1)) * 8192 + vb0; v2u u0, u1, u2, u3; v4u pw; unsigned fv, n; \
      asm volatile("s_mov_b32 %6, %12\n\tds_read_b128 %4, %10\n.Lw%=:\n\tds_read_b32 %5, %7\n\tds_read_b64_tr_b16 %0, %8\n\tds_read_b64_tr_b16 %1, %8 offset:4096\n\t" \
                   "ds_read_b64_tr_b16 %2, %9\n\tds_read_b64_tr_b16 %3, %9 offset:4096\n\ts_sub_u32 %6, %6, 1\n\ts_waitcnt lgkmcnt(0)\n\t" \
                   "v_cmp_le_u32 vcc, %11, %5\n\ts_cbranch_scc1 .Lx%=\n\ts_cbranch_vccz .Lw%=\n.Lx%=:" \
                   : "=&v"(u0), "=&v"(u1), "=&v"(u2), "=&v"(u3), "=&v"(pw), "=&v"(fv), "=&s"(n) \
                   : "v"(L_FULL), "v"(va), "v"(va ^ 32), "v"(pa0 + (t) * 256), "s"(FE - 504 + 8 * (t)), "n"(SPIN_CAP) : "memory", "vcc", "scc"); \
      if (rel) asm volatile("ds_write_b32 %0, %1" ::"v"(L_REL + cw * 4), "v"(NF / 2 + (((t) + 1) >> 1)) : "memory"); \
      t0 = mfma(v4u{u0[0], u0[1], u1[0], u1[1]}, pw, t0); t1 = mfma(v4u{u2[0], u2[1], u3[0], u3[1]}, pw, t1); } while (0)
#pragma unroll 1
    for (int t = 0; t < 58; ++t) {
      lds_wait<1>(L_FULL, FE - 504 + 8 * t);
      STAGE(t, true);
    }
    lds_wait<0>(L_FRDY, 1);
    float fac = *(LAS float*)(L + L_FAC + (i & 3) * 4);
    pa = i < 8 ? W_P + bb * 2048 + ((cw * 512 + grp * 128 + i * 16) ^ (((bb >> 1) & 15) << 7)) : W_END; oc = L_OV + (lane >> 3) * 16;
    PIN(fac); PIN(oa); PIN(tagA); PIN(tagB); PIN(pa); PIN(oc);
#pragma unroll
    for (int t = 58; t < 64; ++t) {
      STAGE(t, false);
    }
    const f32x4 acc = ((i & 4) ? t1 : t0) * fac;
    v4u rec = {cvtpk(acc[0], acc[1]), tagA, cvtpk(acc[2], acc[3]), tagB};
    PUT(rec, oa);
    if (cw == 0 && !PROX) STAMP(6);
    __builtin_amdgcn_sched_barrier(0);
  }
  {
    f32x2 w2f[8];
#pragma unroll
    for (int u = 0; u < 16; ++u) { wf[u] = f32x2{lo16(wv[u >> 2][u & 3]), hi16(wv[u >> 2][u & 3])}; PIN(wf[u]); }
#pragma unroll
    for (int u = 0; u < 8; ++u) { w2f[u] = f32x2{lo16(wv[4 + (u >> 2)][u & 3]), hi16(wv[4 + (u >> 2)][u & 3])}; PIN(w2f[u]); }
    lds_wait<0>(L_HRDY, 1);
    const f32x2 h = *(LAS f32x2*)(L + L_OV + 128 + (lane >> 3) * 8);
#pragma unroll
    for (int p = 0; p < 4; ++p) { y[p] = w2f[p] * h[0] + w2f[4 + p] * h[1]; PIN(y[p]); }
    if (cw == 0 && !PROX) STAMP(10);
    bar();
    const f32x4 c = *(LAS f32x4*)(L + oc);
#pragma unroll
    for (int k = 0; k < 4; ++k)
#pragma unroll
      for (int p = 0; p < 4; ++p) y[p] += wf[4 * k + p] * c[k];
    float z[4];
#pragma unroll
    for (int p = 0; p < 4; ++p) {
      auto sw = __builtin_amdgcn_permlane16_swap(__float_as_uint(y[p >> 1][p & 1]), __float_as_uint(y[2 + (p >> 1)][p & 1]), false, false);
      z[p] = __uint_as_float(sw[0]) + __uint_as_float(sw[1]);
    }
    auto s0 = __builtin_amdgcn_permlane32_swap(__float_as_uint(z[0]), __float_as_uint(z[2]), false, false);
    auto s1 = __builtin_amdgcn_permlane32_swap(__float_as_uint(z[1]), __float_as_uint(z[3]), false, false);
    const float y0 = __uint_as_float(s0[0]) + __uint_as_float(s0[1]), y1 = __uint_as_float(s1[0]) + __uint_as_float(s1[1]);
    PUT(REC(y0 + dpp<ROR8>(y0), y1 + dpp<ROR8>(y1)), pa);
    if (cw == 0) STAMP(8);
    FLUSH();
  }
}

extern "C" void kernel_launch(void* const* d_in, const int* in_sizes, int n_in, void* d_out, int out_size, void* d_ws, size_t ws_size, hipStream_t stream) {
  (void)hipFuncSetAttribute((const void*)fused_decode, hipFuncAttributeMaxDynamicSharedMemorySize, L_END);
  fused_decode<<<NWG, NTHR, L_END, stream>>>((const u16*)d_in[0], (const u16*)d_in[1], (const u16*)d_in[2], (const u16*)d_in[3], (const u16*)d_in[4],
                                             (const u16*)d_in[6], (const u16*)d_in[7], (u16*)d_out, (char*)d_ws);
}
```
